```python
import torch

def _softmax(x, axis=-1):
    return torch.softmax(x, dim=axis)
def _gelu(x):
    return torch.nn.functional.gelu(x, approximate="tanh")
def _silu(x):
    return torch.nn.functional.silu(x)
def _sigmoid(x):
    return torch.sigmoid(x)
def _layernorm(x, axis=-1, eps=1e-5):
    m = x.mean(dim=axis, keepdim=True)
    v = x.var(dim=axis, keepdim=True, unbiased=False)
    return (x - m) / torch.sqrt(v + eps)
def _scatter_add(tgt, idx, src, axis=0):
    return tgt.clone().index_add_(axis, idx, src)
def _segment_sum(src, seg, n):
    out = torch.zeros((n,) + src.shape[1:], dtype=src.dtype)
    return out.index_add_(0, seg, src)
def _pad(x, pw):
    flat = tuple(v for pair in reversed(pw) for v in pair)
    return torch.nn.functional.pad(x, flat)

kernel_name = 'concat_heads__grouped_kv_mqa__ssm_conv_h3__prompt'

def setup_inputs():

    _w = (lambda a: (lambda t: t.to(torch.bfloat16) if t.is_floating_point() else t)(torch.as_tensor(a)))
    def _A_setup():
        B, S, D, H = 1, 2048, 512, 32
        Dh = D // H
        return {
            "x": _w((lambda *s: torch.randn(*s, dtype=torch.bfloat16) * 0.05)(B, S, D)),
            "wq": _w((lambda *s: torch.randn(*s, dtype=torch.bfloat16) * 0.8)(D, H * Dh)),
            "wk": _w((lambda *s: torch.randn(*s, dtype=torch.bfloat16) * 0.8)(D, Dh)),
            "wv": _w((lambda *s: torch.randn(*s, dtype=torch.bfloat16) * 0.05)(D, Dh)),
            "wo": _w((lambda *s: torch.randn(*s, dtype=torch.bfloat16) * 0.05)(H * Dh, D)),
        }
    _A_in = _A_setup()

    def _B_setup():
        B, S, D = 1, 2048, 512
        ks = 4
        return {
            "q": _w((lambda *s: torch.randn(*s, dtype=torch.bfloat16) * 0.05)(B, S, D)),
            "k": _w((lambda *s: torch.randn(*s, dtype=torch.bfloat16) * 0.2)(B, S, D)),
            "v": _w((lambda *s: torch.randn(*s, dtype=torch.bfloat16) * 0.2)(B, S, D)),
            "shift_k": _w((lambda *s: torch.randn(*s, dtype=torch.bfloat16) * 0.2)(D, ks) * 0.5),
            "ssm_k": _w((lambda *s: torch.randn(*s, dtype=torch.bfloat16) * 0.4)(D, S) * (1.0 / S ** 0.5)),
            "ssm_D": _w((lambda *s: torch.randn(*s, dtype=torch.bfloat16) * 0.2)(D)),
        }
    _B_in = _B_setup()

    return {
        "A_x": _A_in["x"],
        "A_wq": _A_in["wq"],
        "A_wk": _A_in["wk"],
        "A_wv": _A_in["wv"],
        "A_wo": _A_in["wo"],
        "B_q": _B_in["q"],
        "B_k": _B_in["k"],
        "B_v": _B_in["v"],
        "B_shift_k": _B_in["shift_k"],
        "B_ssm_k": _B_in["ssm_k"],
        "B_ssm_D": _B_in["ssm_D"],
    }

def reference(A_x, A_wq, A_wk, A_wv, A_wo, B_q, B_k, B_v, B_shift_k, B_ssm_k, B_ssm_D):
    def _A_ref(x, wq, wk, wv, wo):
        B, S, D = x.shape
        H = 32
        Dh = D // H
        q = (lambda x, *d: x.permute(*d))((x @ wq).reshape(B, S, H, Dh), 0, 2, 1, 3)
        k = x @ wk
        v = x @ wv
        scores = torch.einsum("bhqd,bkd->bhqk", q, k) * (Dh ** -0.5)
        p = _softmax(scores, axis=-1)
        o = torch.einsum("bhqk,bkd->bhqd", p, v)
        return (lambda x, *d: x.permute(*d))(o, 0, 2, 1, 3).reshape(B, S, H * Dh) @ wo

    def _B_ref(q, k, v, shift_k, ssm_k, ssm_D):
        B, S, D = q.shape
        ks = shift_k.shape[-1]
        k_pad = _pad(k, ((0, 0), (ks - 1, 0), (0, 0)))
        k_shift = torch.zeros_like(k)
        for t in range(ks):
            k_shift = k_shift + k_pad[:, t:t + S, :] * shift_k[:, ks - 1 - t][None, None, :]
        kv = k_shift * v
        L = 2 * S
        kv_pad = _pad(kv, ((0, 0), (0, S), (0, 0)))
        kf_pad = _pad(ssm_k, ((0, 0), (0, S)))
        kv_f = (lambda x, axes=(-2,-1): torch.fft.rfft2(x.float(), dim=axes))(kv_pad[:, None, :, :], axes=(1, 2))
        kf_f = (lambda x, axes=(-2,-1): torch.fft.rfft2(x.float(), dim=axes))(kf_pad[:, None, :], axes=(1, 2))
        kf_f = (lambda x, *d: x.permute(*d))(kf_f, 1, 2, 0)
        y_f = kv_f * kf_f[None, :, :, :]
        y = (lambda x, s, axes=(-2,-1): torch.fft.irfft2(x, s=s, dim=axes).to(torch.bfloat16))(y_f, (1, L), axes=(1, 2))[:, 0, :S, :]
        y = y + kv * ssm_D[None, None, :]
        return q * y

    a = _A_ref(A_x, A_wq, A_wk, A_wv, A_wo)
    b = _B_ref(B_q, B_k, B_v, B_shift_k, B_ssm_k, B_ssm_D)
    return torch.concatenate([a, b], axis=-1)
```

```cpp
#include <hip/hip_runtime.h>
#include <hip/hip_bf16.h>
#include <stdint.h>

typedef __attribute__((ext_vector_type(2))) __bf16 bf16x2;
typedef __attribute__((ext_vector_type(8))) __bf16 bf16x8;
typedef __attribute__((ext_vector_type(16))) __bf16 bf16x16;
typedef __attribute__((ext_vector_type(2))) float f32x2;
typedef __attribute__((ext_vector_type(4))) float f32x4;
typedef __attribute__((ext_vector_type(8))) float f32x8;
typedef __attribute__((ext_vector_type(16))) float f32x16;
typedef __attribute__((ext_vector_type(2))) unsigned u32x2;
typedef __attribute__((ext_vector_type(4))) unsigned u32x4;
typedef __attribute__((ext_vector_type(4))) short s16x4;
typedef unsigned long long u64;
#define LAS __attribute__((address_space(3)))
#define GAS __attribute__((address_space(1)))
#define CAS __attribute__((address_space(4)))
#define DEVI __device__ __forceinline__

constexpr unsigned WS_EPOCH = 0;
constexpr unsigned WS_FA = 64u << 10;
constexpr unsigned WS_FB = 128u << 10;
constexpr unsigned WS_FY = 256u << 10;
constexpr unsigned WS_FO = 512u << 10;
constexpr unsigned WS_KV = 384u << 10;
constexpr unsigned WS_U = 1u << 20;
constexpr unsigned WS_Z = 3u << 20;
constexpr unsigned WS_O = 5u << 20;
constexpr unsigned WS_END = 7u << 20;

constexpr unsigned L_X = 0, L_WQ = 65536, L_KV = 0;
constexpr unsigned L_O = 32768;
constexpr unsigned L_CV = 131072;
constexpr unsigned L_WP = L_CV, L_QX = L_CV + 19200, L_T = L_CV + 17152, T_WAVE = 2304, L_SH = L_CV + 27392;
constexpr unsigned L_A = L_CV, A_COPY = 4288, A_CH = 2 * A_COPY;
constexpr unsigned L_U = L_CV + 2 * A_CH, U_ROW = 144;
constexpr unsigned L_ZT = L_CV, L_OX = L_CV + 8192;
constexpr unsigned L_MISC = 163584;
constexpr unsigned C_CS = L_MISC, W_KMAX = L_MISC + 12, C_FULL = L_MISC + 16, C_HALF = L_MISC + 20, C_FULL2 = L_MISC + 24,
                   C_CD = L_MISC + 28, C_CP = L_MISC + 32, C_B1 = L_MISC + 40, C_FB = L_MISC + 44, F_AP = L_MISC + 48,
                   C_FY = L_MISC + 84, C_YJ = L_MISC + 92, C_OD = L_MISC + 96, F_OL = L_MISC + 100, W_EPOCH = L_MISC + 120;
constexpr unsigned L_TOTAL = 163840;

constexpr unsigned SPIN_CAP = 40000;
constexpr unsigned OOB = 0x80000000u;
constexpr float QSCALE = 0.25f * 1.4426950408889634f;

DEVI void dma16(const void* base, unsigned voff, unsigned lds) {
  unsigned keep;
  asm volatile("s_mov_b32 %0, m0\n\ts_mov_b32 m0, %3\n\ts_nop 0\n\tglobal_load_lds_dwordx4 %1, %2\n\ts_mov_b32 m0, %0"
               : "=&s"(keep) : "v"(voff), "s"(base), "s"(lds) : "memory");
}
DEVI void dma16_dev(const void* base, unsigned voff, unsigned lds) {
  unsigned keep;
  asm volatile("s_mov_b32 %0, m0\n\ts_mov_b32 m0, %3\n\ts_nop 0\n\tglobal_load_lds_dwordx4 %1, %2 sc1\n\ts_mov_b32 m0, %0"
               : "=&s"(keep) : "v"(voff), "s"(base), "s"(lds) : "memory");
}
DEVI void dma16x4(const void* base, unsigned voff, unsigned lds) {
  unsigned keep;
  asm volatile("s_mov_b32 %0, m0\n\ts_mov_b32 m0, %3\n\ts_nop 0\n\tglobal_load_lds_dwordx4 %1, %2\n\tglobal_load_lds_dwordx4 %1, %2 offset:1024\n\t"
               "global_load_lds_dwordx4 %1, %2 offset:2048\n\tglobal_load_lds_dwordx4 %1, %2 offset:3072\n\ts_mov_b32 m0, %0"
               : "=&s"(keep) : "v"(voff), "s"(base), "s"(lds) : "memory");
}
DEVI void dma16x4_dev(const void* base, unsigned voff, unsigned lds) {
  unsigned keep;
  asm volatile("s_mov_b32 %0, m0\n\ts_mov_b32 m0, %3\n\ts_nop 0\n\tglobal_load_lds_dwordx4 %1, %2 sc1\n\tglobal_load_lds_dwordx4 %1, %2 offset:1024 sc1\n\t"
               "global_load_lds_dwordx4 %1, %2 offset:2048 sc1\n\tglobal_load_lds_dwordx4 %1, %2 offset:3072 sc1\n\ts_mov_b32 m0, %0"
               : "=&s"(keep) : "v"(voff), "s"(base), "s"(lds) : "memory");
}
DEVI void dma16_buf(__amdgpu_buffer_rsrc_t r, unsigned voff, unsigned lds) {
  unsigned keep;
  asm volatile("s_mov_b32 %0, m0\n\ts_mov_b32 m0, %3\n\ts_nop 0\n\tbuffer_load_dwordx4 %1, %2, 0 offen lds\n\ts_mov_b32 m0, %0"
               : "=&s"(keep) : "v"(voff), "s"(r), "s"(lds) : "memory");
}
DEVI void wait_vm0() { asm volatile("s_waitcnt vmcnt(0)" ::: "memory"); }
DEVI void wait_lgkm0() { asm volatile("s_waitcnt lgkmcnt(0)" ::: "memory"); }
DEVI void barrier() { asm volatile("s_waitcnt lgkmcnt(0)\n\ts_barrier" ::: "memory"); }

DEVI u32x4 lds_rd128(unsigned a) { return *(LAS u32x4*)(uintptr_t)a; }
DEVI u32x2 lds_rd64(unsigned a) { return *(LAS u32x2*)(uintptr_t)a; }
DEVI unsigned lds_rd32(unsigned a) { return *(LAS unsigned*)(uintptr_t)a; }
DEVI void lds_wr128(unsigned a, u32x4 v) { *(LAS u32x4*)(uintptr_t)a = v; }
DEVI u32x2 lds_tr(unsigned a) {
  return __builtin_bit_cast(u32x2, __builtin_amdgcn_ds_read_tr16_b64_v4i16((LAS s16x4*)(uintptr_t)a));
}
DEVI u32x4 lds_tr2(unsigned a, unsigned d) { const u32x2 x = lds_tr(a), y = lds_tr(a + d); return u32x4{x.x, x.y, y.x, y.y}; }
DEVI bf16x8 as_bf8(u32x4 v) { return __builtin_bit_cast(bf16x8, v); }
DEVI unsigned pack2(float lo, float hi) {
  f32x2 f = {lo, hi};
  return __builtin_bit_cast(unsigned, __builtin_convertvector(f, bf16x2));
}
DEVI float bf_lo(unsigned u) { return __uint_as_float(u << 16); }
DEVI float bf_hi(unsigned u) { return __uint_as_float(u & 0xffff0000u); }
DEVI float rnd_bf(float x) { return bf_lo(pack2(x, x)); }

template <class T> DEVI void pin(T& x) { asm volatile("" : "+v"(x)); }
DEVI void fence_sched() { __builtin_amdgcn_sched_barrier(0); }
DEVI float sum_halves(float v) {
  auto r = __builtin_amdgcn_permlane32_swap(__float_as_uint(v), __float_as_uint(v), false, false);
  return __uint_as_float(r[0]) + __uint_as_float(r[1]);
}
DEVI float sum_rows(float v) {
  auto r = __builtin_amdgcn_permlane16_swap(__float_as_uint(v), __float_as_uint(v), false, false);
  return __uint_as_float(r[0]) + __uint_as_float(r[1]);
}
DEVI unsigned max_in_row(unsigned v) {
  v = max(v, (unsigned)__builtin_amdgcn_update_dpp(0, (int)v, 0x128, 0xf, 0xf, false));
  v = max(v, (unsigned)__builtin_amdgcn_update_dpp(0, (int)v, 0x124, 0xf, 0xf, false));
  v = max(v, (unsigned)__builtin_amdgcn_update_dpp(0, (int)v, 0x122, 0xf, 0xf, false));
  v = max(v, (unsigned)__builtin_amdgcn_update_dpp(0, (int)v, 0x121, 0xf, 0xf, false));
  return v;
}
DEVI float sq8(u32x4 q) {
  return bf_lo(q.x) * bf_lo(q.x) + bf_hi(q.x) * bf_hi(q.x) + bf_lo(q.y) * bf_lo(q.y) + bf_hi(q.y) * bf_hi(q.y) +
         bf_lo(q.z) * bf_lo(q.z) + bf_hi(q.z) * bf_hi(q.z) + bf_lo(q.w) * bf_lo(q.w) + bf_hi(q.w) * bf_hi(q.w);
}

typedef __amdgpu_buffer_rsrc_t rsrc_t;
DEVI rsrc_t mk_rsrc(const void* p, unsigned bytes) {
  return __builtin_amdgcn_make_buffer_rsrc((void*)p, 0, (int)bytes, 0x00020000);
}
DEVI u32x4 buf_ld128(rsrc_t r, unsigned off, unsigned so = 0) { return __builtin_amdgcn_raw_buffer_load_b128(r, (int)off, (int)so, 0); }
DEVI u32x4 buf_ld128_dev(rsrc_t r, unsigned off, unsigned so = 0) { return __builtin_amdgcn_raw_buffer_load_b128(r, (int)off, (int)so, 16); }
DEVI u32x2 buf_ld64_dev(rsrc_t r, unsigned off) { return __builtin_amdgcn_raw_buffer_load_b64(r, (int)off, 0, 16); }
DEVI void buf_st128(rsrc_t r, unsigned off, unsigned so, u32x4 v) { __builtin_amdgcn_raw_buffer_store_b128(v, r, (int)off, (int)so, 0); }
DEVI void buf_st128_dev(rsrc_t r, unsigned off, unsigned so, u32x4 v) { __builtin_amdgcn_raw_buffer_store_b128(v, r, (int)off, (int)so, 16); }
DEVI void buf_st64_dev(rsrc_t r, unsigned off, u32x2 v) { __builtin_amdgcn_raw_buffer_store_b64(v, r, (int)off, 0, 16); }

DEVI void store_flag(rsrc_t ws, unsigned off, u64 word) { buf_st64_dev(ws, off, u32x2{(unsigned)word, (unsigned)(word >> 32)}); }
DEVI bool tag_ok(u32x2 f, u64 epoch) { return ((f.x >> 16) == (unsigned)(epoch & 0xffffu)) & (f.y == (unsigned)(epoch >> 16)); }
template <int N, int NAP>
DEVI unsigned wait_flags(rsrc_t ws, unsigned off, unsigned stride, u64 epoch) {
  unsigned payload = 0;
#pragma unroll 1
  for (unsigned spin = 0; spin < SPIN_CAP; ++spin) {
    asm volatile("" ::: "memory");
    bool ok = true;
    payload = 0;
#pragma unroll
    for (int i = 0; i < N; ++i) {
      const u32x2 f = buf_ld64_dev(ws, off + i * stride);
      ok &= tag_ok(f, epoch);
      payload = max(payload, f.x & 0xffffu);
    }
    if (__all(ok)) break;
    __builtin_amdgcn_s_sleep(NAP);
  }
  asm volatile("" ::: "memory");
  return payload;
}
DEVI void lds_signal(unsigned a, int) {
  asm volatile("s_waitcnt lgkmcnt(0)\n\ts_mov_b64 exec, 1\n\tds_add_u32 %0, %1\n\ts_mov_b64 exec, -1" :: "v"(a), "v"(1u) : "memory");
}
DEVI void publish_join(rsrc_t ws, unsigned cnt, unsigned waves, unsigned flag_off, u64 word, int lane) {
  wait_vm0();
  if (lane == 0 && __hip_atomic_fetch_add((LAS unsigned*)(uintptr_t)cnt, 1u, __ATOMIC_RELAXED, __HIP_MEMORY_SCOPE_WORKGROUP) == waves - 1)
    store_flag(ws, flag_off, word);
}
template <int NAP>
DEVI void lds_wait(unsigned a, unsigned want) {
  unsigned v, sv, left = 64 * SPIN_CAP;
  if (NAP)
    asm volatile("1:\n\tds_read_b32 %0, %3\n\ts_waitcnt lgkmcnt(0)\n\tv_readfirstlane_b32 %1, %0\n\ts_cmp_ge_u32 %1, %4\n\ts_cbranch_scc1 2f\n\t"
                 "s_sleep %5\n\ts_sub_u32 %2, %2, 1\n\ts_cbranch_scc0 1b\n\t2:"
                 : "=&v"(v), "=&s"(sv), "+s"(left) : "v"(a), "i"(want), "i"(NAP) : "memory", "scc");
  else
    asm volatile("1:\n\tds_read_b32 %0, %3\n\ts_waitcnt lgkmcnt(0)\n\tv_readfirstlane_b32 %1, %0\n\ts_cmp_ge_u32 %1, %4\n\ts_cbranch_scc1 2f\n\t"
                 "s_sub_u32 %2, %2, 1\n\ts_cbranch_scc0 1b\n\t2:"
                 : "=&v"(v), "=&s"(sv), "+s"(left) : "v"(a), "i"(want) : "memory", "scc");
}

DEVI u64 lds_epoch() {
  const u32x2 e = lds_rd64(W_EPOCH);
  return ((u64)__builtin_amdgcn_readfirstlane(e.y) << 32) | (unsigned)__builtin_amdgcn_readfirstlane(e.x);
}

#define STAMP(k)
#define STAMP_DUMP()

struct Params {
  const void *x, *wq, *wk, *wv, *wo, *bq, *bk, *bv, *shk, *ssk, *ssd;
  void *out, *ws;
};

__global__ __launch_bounds__(768) void fused_kernel(Params p) {
  __shared__ __attribute__((aligned(16))) char smem[L_TOTAL];
  asm volatile("" :: "v"((LAS char*)smem) : "memory");
  const int tid = threadIdx.x, lane = tid & 63;
  const int wave = __builtin_amdgcn_readfirstlane(tid >> 6);
  const int b = blockIdx.x, xcd = b & 7, j = b >> 3;
  const int qb = 4 * xcd + (j >> 3), hg = j & 7;
  const rsrc_t ws = mk_rsrc(p.ws, WS_END);
  const int l15 = lane & 15, g4 = lane >> 4, l31 = lane & 31, hh = lane >> 5;
  const int tq = l15 >> 2, tp = lane & 3;
  const int blk = (hg >> 1) & 1, isv = hg >> 2, crit = 32 * (hg & 1) + 16 * blk;
  {
    u64 pc;
    unsigned keep;
    asm volatile("s_getpc_b64 %0\n\ts_mov_b32 %1, m0\n\ts_mov_b32 m0, %3\n\ts_nop 0\n\tglobal_load_lds_dword %2, %0\n\ts_mov_b32 m0, %1"
                 : "=&s"(pc), "=&s"(keep) : "v"(min(tid * 32, 22400)), "s"(L_CV + 31232) : "memory");
  }
  STAMP(0);
  if (wave < 8) {
    if (tid < 32) *(LAS unsigned*)(uintptr_t)(L_MISC + tid * 4) = 0u;
    barrier();
    {
      const char* xb = (const char*)p.x + (size_t)qb * 65536;
      const char* wb = (const char*)p.wq + hg * 128;
#pragma unroll
      for (int i = 0; i < 6; ++i) {
        const int n = wave * 6 + i, row = n < crit ? n : n + 16;
        dma16(xb + row * 1024, (lane ^ (row & 15)) << 4, L_X + row * 1024);
      }
      const int wr = lane >> 3, wsw = ((wr >> 1) & 1) | ((wave & 1) << 1);
      const unsigned wo = wr * 1024 + (((lane & 7) ^ (wsw << 1)) << 4);
#pragma unroll
      for (int i = 0; i < 8; ++i) dma16(wb + (i * 8 + wave) * 8192, wo, L_WQ + (i * 8 + wave) * 1024);
    }
    STAMP(1);
    const int qi = (wave >> 1) & 1, kh = wave & 1, hl = 2 * (wave >> 2) + kh;
    f32x4 d0 = {}, d1 = {};
    {
      unsigned wa = L_WQ + (8 * g4 + tq) * 128 + (((2 * hl + (tp >> 1)) ^ ((((tq >> 1) & 1) | ((g4 & 1) << 1)) << 1)) << 4) + 8 * (tp & 1);
      unsigned xa[4];
#pragma unroll
      for (int i = 0; i < 4; ++i) {
        xa[i] = L_X + (32 * qi + l15) * 1024 + (((4 * i + g4) ^ l15) << 4);
        pin(xa[i]);
      }
      pin(wa);
      asm volatile("s_waitcnt vmcnt(4)" ::: "memory");
      STAMP(2);
      barrier();
      STAMP(4);
      u32x4 wf[2][4], xf[2][8];
      auto load = [&](int g) {
#pragma unroll
        for (int i = 0; i < 4; ++i) {
          wf[g & 1][i] = lds_tr2(wa + (4 * g + i) * 4096, 512);
          xf[g & 1][2 * i] = lds_rd128(xa[i] + g * 256);
          xf[g & 1][2 * i + 1] = lds_rd128(xa[i] + g * 256 + 16384);
        }
      };
      load(0);
#pragma unroll
      for (int g = 0; g < 4; ++g) {
        if (g == 1) {
          wait_vm0();
          STAMP(3);
          asm volatile("s_barrier" ::: "memory");
        }
        if (g < 3) load(g + 1);
        fence_sched();
#pragma unroll
        for (int i = 0; i < 4; ++i) {
          const bf16x8 w8 = as_bf8(wf[g & 1][i]);
          d0 = __builtin_amdgcn_mfma_f32_16x16x32_bf16(w8, as_bf8(xf[g & 1][2 * i]), d0, 0, 0, 0);
          d1 = __builtin_amdgcn_mfma_f32_16x16x32_bf16(w8, as_bf8(xf[g & 1][2 * i + 1]), d1, 0, 0, 0);
        }
        pin(d0);
        pin(d1);
        fence_sched();
      }
    }
    u32x4 qown;
    {
      auto s0 = __builtin_amdgcn_permlane16_swap(pack2(d0[0] * QSCALE, d0[1] * QSCALE), pack2(d1[0] * QSCALE, d1[1] * QSCALE), false, false);
      auto s1 = __builtin_amdgcn_permlane16_swap(pack2(d0[2] * QSCALE, d0[3] * QSCALE), pack2(d1[2] * QSCALE, d1[3] * QSCALE), false, false);
      qown = u32x4{s0[0], s1[0], s0[1], s1[1]};
    }
    lds_wr128(L_QX + wave * 1024 + lane * 16, qown);
    STAMP(5);
    barrier();
    STAMP(6);
    {
      const unsigned off = (wave & 4) * 16384 + (wave & 3) * 4096;
      dma16x4_dev((const char*)p.ws + WS_KV + off, lane * 16, L_KV + off);
    }
    const u32x4 qoth = lds_rd128(L_QX + (wave ^ 1) * 1024 + lane * 16);
    const float negm = -1.001f * __builtin_amdgcn_sqrtf(fmaxf(sum_halves(sq8(qown)), sum_halves(sq8(qoth)))) * __uint_as_float(lds_rd32(W_KMAX));
    const u64 epoch = lds_epoch();

    f32x16 oa = {}, ob = {};
    unsigned xo = L_OX + wave * 2304 + lane * 16, yo = L_OX + (wave ^ 1) * 2304 + lane * 16;
    unsigned xl = L_OX + wave * 2304 + 2048 + lane * 4, yl = L_OX + (wave ^ 1) * 2304 + 2048 + lane * 4;
    unsigned oo = WS_O + ((2 * qb + qi) * 32 + 4 * hg + hl) * 1024 + lane * 16;
    {
      unsigned cnt, t0, t1, t2, tv;
      unsigned ka = L_KV + kh * 65536 + lane * 16, va = L_KV + kh * 65536 + 1024 + hh * 256 + l15 * 16;
      const unsigned one = (lane & 27) == 16 ? 0x3f803f80u : 0u;
      u32x4 kf, v00 = {one, one, one, one}, v01 = v00, v10 = v00, v11 = v00;
      f32x16 cinit;
#pragma unroll
      for (int i = 0; i < 16; ++i) cinit[i] = negm;
      pin(cinit); pin(oa); pin(ob); pin(v00); pin(v01); pin(v10); pin(v11);
      pin(xo); pin(yo); pin(xl); pin(yl); pin(oo); pin(ka); pin(va);
      wait_vm0();
      STAMP(7);
      barrier();
      STAMP(9);
      asm volatile(
          "s_movk_i32 %[t2], 410\n\t"
          "ds_read_b128 %[k], %[ka]\n\t"
          "s_mov_b64 exec, %[msk]\n\t"
          "ds_read_b128 %[v00], %[va]\n\t"
          "ds_read_b128 %[v01], %[va] offset:512\n\t"
          "s_mov_b64 exec, -1\n\t"
          "v_mov_b32 v56, 0\n\t"
          "v_mov_b32 v57, 0\n\t"
          "v_mov_b32 v58, 0\n\t"
          "v_mov_b32 v59, 0\n\t"
          "v_mov_b32 v60, 0\n\t"
          "v_mov_b32 v61, 0\n\t"
          "v_mov_b32 v62, 0\n\t"
          "v_mov_b32 v63, 0\n\t"
          "s_mov_b32 %[cnt], 0\n\t"
          "s_setprio 2\n\t"
          "s_waitcnt lgkmcnt(0)\n\t"
          "v_mfma_f32_32x32x16_bf16 v[32:47], %[k], %[qa], %[c]\n\t"
          "s_nop 11\n\t"
          "1:\n\t"
          "v_exp_f32 v32, v32\n\t"
          "v_exp_f32 v33, v33\n\t"
          "v_smfmac_f32_32x32x32_bf16 %[ob], %[v11], v[56:63], %[idx]\n\t"
          "v_exp_f32 v34, v34\n\t"
          "v_exp_f32 v35, v35\n\t"
          "v_exp_f32 v36, v36\n\t"
          "v_mfma_f32_32x32x16_bf16 v[48:63], %[k], %[qb], %[c]\n\t"
          "ds_read_b128 %[k], %[ka] offset:2048\n\t"
          "s_mov_b64 exec, %[msk]\n\t"
          "ds_read_b128 %[v10], %[va] offset:2048\n\t"
          "ds_read_b128 %[v11], %[va] offset:2560\n\t"
          "s_mov_b64 exec, -1\n\t"
          "v_exp_f32 v37, v37\n\t"
          "v_exp_f32 v38, v38\n\t"
          "v_exp_f32 v39, v39\n\t"
          "v_exp_f32 v40, v40\n\t"
          "v_exp_f32 v41, v41\n\t"
          "v_smfmac_f32_32x32x32_bf16 %[oa], %[v00], v[32:39], %[idx]\n\t"
          "v_exp_f32 v42, v42\n\t"
          "v_exp_f32 v43, v43\n\t"
          "v_exp_f32 v44, v44\n\t"
          "v_exp_f32 v45, v45\n\t"
          "v_exp_f32 v46, v46\n\t"
          "v_exp_f32 v47, v47\n\t"
          "v_exp_f32 v48, v48\n\t"
          "v_exp_f32 v49, v49\n\t"
          "v_smfmac_f32_32x32x32_bf16 %[oa], %[v01], v[40:47], %[idx]\n\t"
          "v_exp_f32 v50, v50\n\t"
          "v_exp_f32 v51, v51\n\t"
          "v_exp_f32 v52, v52\n\t"
          "s_waitcnt lgkmcnt(0)\n\t"
          "v_mfma_f32_32x32x16_bf16 v[32:47], %[k], %[qa], %[c]\n\t"
          "v_exp_f32 v53, v53\n\t"
          "v_exp_f32 v54, v54\n\t"
          "v_exp_f32 v55, v55\n\t"
          "v_exp_f32 v56, v56\n\t"
          "v_exp_f32 v57, v57\n\t"
          "v_smfmac_f32_32x32x32_bf16 %[ob], %[v00], v[48:55], %[idx]\n\t"
          "v_exp_f32 v58, v58\n\t"
          "v_exp_f32 v59, v59\n\t"
          "v_exp_f32 v60, v60\n\t"
          "v_exp_f32 v61, v61\n\t"
          "v_exp_f32 v62, v62\n\t"
          "v_exp_f32 v63, v63\n\t"
          "v_exp_f32 v32, v32\n\t"
          "v_exp_f32 v33, v33\n\t"
          "v_smfmac_f32_32x32x32_bf16 %[ob], %[v01], v[56:63], %[idx]\n\t"
          "v_exp_f32 v34, v34\n\t"
          "v_exp_f32 v35, v35\n\t"
          "v_exp_f32 v36, v36\n\t"
          "v_mfma_f32_32x32x16_bf16 v[48:63], %[k], %[qb], %[c]\n\t"
          "ds_read_b128 %[k], %[ka] offset:4096\n\t"
          "s_mov_b64 exec, %[msk]\n\t"
          "ds_read_b128 %[v00], %[va] offset:4096\n\t"
          "ds_read_b128 %[v01], %[va] offset:4608\n\t"
          "s_mov_b64 exec, -1\n\t"
          "v_exp_f32 v37, v37\n\t"
          "v_exp_f32 v38, v38\n\t"
          "v_exp_f32 v39, v39\n\t"
          "v_exp_f32 v40, v40\n\t"
          "v_exp_f32 v41, v41\n\t"
          "v_smfmac_f32_32x32x32_bf16 %[oa], %[v10], v[32:39], %[idx]\n\t"
          "v_exp_f32 v42, v42\n\t"
          "v_exp_f32 v43, v43\n\t"
          "v_exp_f32 v44, v44\n\t"
          "v_exp_f32 v45, v45\n\t"
          "v_exp_f32 v46, v46\n\t"
          "v_exp_f32 v47, v47\n\t"
          "v_exp_f32 v48, v48\n\t"
          "v_exp_f32 v49, v49\n\t"
          "v_smfmac_f32_32x32x32_bf16 %[oa], %[v11], v[40:47], %[idx]\n\t"
          "v_exp_f32 v50, v50\n\t"
          "v_exp_f32 v51, v51\n\t"
          "v_exp_f32 v52, v52\n\t"
          "s_waitcnt lgkmcnt(0)\n\t"
          "v_mfma_f32_32x32x16_bf16 v[32:47], %[k], %[qa], %[c]\n\t"
          "v_exp_f32 v53, v53\n\t"
          "v_exp_f32 v54, v54\n\t"
          "v_exp_f32 v55, v55\n\t"
          "v_exp_f32 v56, v56\n\t"
          "v_exp_f32 v57, v57\n\t"
          "v_smfmac_f32_32x32x32_bf16 %[ob], %[v10], v[48:55], %[idx]\n\t"
          "v_exp_f32 v58, v58\n\t"
          "v_exp_f32 v59, v59\n\t"
          "v_exp_f32 v60, v60\n\t"
          "v_exp_f32 v61, v61\n\t"
          "v_exp_f32 v62, v62\n\t"
          "v_exp_f32 v63, v63\n\t"
          "v_exp_f32 v32, v32\n\t"
          "v_exp_f32 v33, v33\n\t"
          "v_smfmac_f32_32x32x32_bf16 %[ob], %[v11], v[56:63], %[idx]\n\t"
          "v_exp_f32 v34, v34\n\t"
          "v_exp_f32 v35, v35\n\t"
          "v_exp_f32 v36, v36\n\t"
          "v_mfma_f32_32x32x16_bf16 v[48:63], %[k], %[qb], %[c]\n\t"
          "ds_read_b128 %[k], %[ka] offset:6144\n\t"
          "s_mov_b64 exec, %[msk]\n\t"
          "ds_read_b128 %[v10], %[va] offset:6144\n\t"
          "ds_read_b128 %[v11], %[va] offset:6656\n\t"
          "s_mov_b64 exec, -1\n\t"
          "v_exp_f32 v37, v37\n\t"
          "v_exp_f32 v38, v38\n\t"
          "v_exp_f32 v39, v39\n\t"
          "v_exp_f32 v40, v40\n\t"
          "v_exp_f32 v41, v41\n\t"
          "v_smfmac_f32_32x32x32_bf16 %[oa], %[v00], v[32:39], %[idx]\n\t"
          "v_exp_f32 v42, v42\n\t"
          "v_exp_f32 v43, v43\n\t"
          "v_exp_f32 v44, v44\n\t"
          "v_exp_f32 v45, v45\n\t"
          "v_exp_f32 v46, v46\n\t"
          "v_exp_f32 v47, v47\n\t"
          "v_exp_f32 v48, v48\n\t"
          "v_exp_f32 v49, v49\n\t"
          "v_smfmac_f32_32x32x32_bf16 %[oa], %[v01], v[40:47], %[idx]\n\t"
          "v_exp_f32 v50, v50\n\t"
          "v_exp_f32 v51, v51\n\t"
          "v_exp_f32 v52, v52\n\t"
          "s_waitcnt lgkmcnt(0)\n\t"
          "v_mfma_f32_32x32x16_bf16 v[32:47], %[k], %[qa], %[c]\n\t"
          "v_exp_f32 v53, v53\n\t"
          "v_exp_f32 v54, v54\n\t"
          "v_exp_f32 v55, v55\n\t"
          "v_exp_f32 v56, v56\n\t"
          "v_exp_f32 v57, v57\n\t"
          "v_smfmac_f32_32x32x32_bf16 %[ob], %[v00], v[48:55], %[idx]\n\t"
          "v_exp_f32 v58, v58\n\t"
          "v_exp_f32 v59, v59\n\t"
          "v_exp_f32 v60, v60\n\t"
          "v_exp_f32 v61, v61\n\t"
          "v_exp_f32 v62, v62\n\t"
          "v_exp_f32 v63, v63\n\t"
          "v_exp_f32 v32, v32\n\t"
          "v_exp_f32 v33, v33\n\t"
          "v_smfmac_f32_32x32x32_bf16 %[ob], %[v01], v[56:63], %[idx]\n\t"
          "v_exp_f32 v34, v34\n\t"
          "v_exp_f32 v35, v35\n\t"
          "v_exp_f32 v36, v36\n\t"
          "v_mfma_f32_32x32x16_bf16 v[48:63], %[k], %[qb], %[c]\n\t"
          "ds_read_b128 %[k], %[ka] offset:8192\n\t"
          "s_mov_b64 exec, %[msk]\n\t"
          "ds_read_b128 %[v00], %[va] offset:8192\n\t"
          "ds_read_b128 %[v01], %[va] offset:8704\n\t"
          "s_mov_b64 exec, -1\n\t"
          "v_exp_f32 v37, v37\n\t"
          "v_exp_f32 v38, v38\n\t"
          "v_exp_f32 v39, v39\n\t"
          "v_exp_f32 v40, v40\n\t"
          "v_exp_f32 v41, v41\n\t"
          "v_smfmac_f32_32x32x32_bf16 %[oa], %[v10], v[32:39], %[idx]\n\t"
          "v_exp_f32 v42, v42\n\t"
          "v_exp_f32 v43, v43\n\t"
          "v_exp_f32 v44, v44\n\t"
          "v_exp_f32 v45, v45\n\t"
          "v_exp_f32 v46, v46\n\t"
          "v_exp_f32 v47, v47\n\t"
          "v_exp_f32 v48, v48\n\t"
          "v_exp_f32 v49, v49\n\t"
          "v_smfmac_f32_32x32x32_bf16 %[oa], %[v11], v[40:47], %[idx]\n\t"
          "v_exp_f32 v50, v50\n\t"
          "v_exp_f32 v51, v51\n\t"
          "v_exp_f32 v52, v52\n\t"
          "s_waitcnt lgkmcnt(0)\n\t"
          "v_mfma_f32_32x32x16_bf16 v[32:47], %[k], %[qa], %[c]\n\t"
          "v_exp_f32 v53, v53\n\t"
          "v_exp_f32 v54, v54\n\t"
          "v_exp_f32 v55, v55\n\t"
          "v_exp_f32 v56, v56\n\t"
          "v_exp_f32 v57, v57\n\t"
          "v_smfmac_f32_32x32x32_bf16 %[ob], %[v10], v[48:55], %[idx]\n\t"
          "v_exp_f32 v58, v58\n\t"
          "v_exp_f32 v59, v59\n\t"
          "v_exp_f32 v60, v60\n\t"
          "v_exp_f32 v61, v61\n\t"
          "v_exp_f32 v62, v62\n\t"
          "v_exp_f32 v63, v63\n\t"
          "v_add_u32 %[ka], 8192, %[ka]\n\t"
          "v_add_u32 %[va], 8192, %[va]\n\t"
          "s_add_u32 %[cnt], %[cnt], 1\n\t"
          "s_bitcmp0_b32 %[t2], %[cnt]\n\t"
          "s_cbranch_scc1 1b\n\t"
          "s_cmp_lg_u32 %[cnt], 4\n\t"
          "s_cbranch_scc1 7f\n\t"
          "s_setprio 1\n\t"
          "7:\n\t"
          "s_cmp_lg_u32 %[cnt], 7\n\t"
          "s_cbranch_scc1 8f\n\t"
          "s_setprio 0\n\t"
          "8:\n\t"
          "s_cmp_lg_u32 %[cnt], 1\n\t"
          "s_cbranch_scc1 3f\n\t"
          "s_mov_b32 %[t0], 0\n\t"
          "2:\n\t"
          "ds_read_b32 %[tv], %[cfull] offset:0\n\t"
          "s_waitcnt lgkmcnt(0)\n\t"
          "v_readfirstlane_b32 %[t1], %[tv]\n\t"
          "s_add_u32 %[t0], %[t0], 1\n\t"
          "s_cmp_ge_u32 %[t1], 4\n\t"
          "s_cbranch_scc1 1b\n\t"
          "s_sleep 1\n\t"
          "s_cmp_lt_u32 %[t0], 0x100000\n\t"
          "s_cbranch_scc1 2b\n\t"
          "s_branch 1b\n\t"
          "3:\n\t"
          "s_cmp_lg_u32 %[cnt], 4\n\t"
          "s_cbranch_scc1 4f\n\t"
          "v_mov_b32 %[tv], 1\n\t"
          "s_mov_b64 exec, 1\n\t"
          "ds_add_u32 %[cfull], %[tv] offset:4\n\t"
          "s_mov_b64 exec, -1\n\t"
          "4:\n\t"
          "s_cmp_lg_u32 %[cnt], 3\n\t"
          "s_cbranch_scc1 5f\n\t"
          "s_mov_b32 %[t0], 0\n\t"
          "6:\n\t"
          "ds_read_b32 %[tv], %[cfull] offset:8\n\t"
          "s_waitcnt lgkmcnt(0)\n\t"
          "v_readfirstlane_b32 %[t1], %[tv]\n\t"
          "s_add_u32 %[t0], %[t0], 1\n\t"
          "s_cmp_ge_u32 %[t1], 4\n\t"
          "s_cbranch_scc1 1b\n\t"
          "s_sleep 1\n\t"
          "s_cmp_lt_u32 %[t0], 0x100000\n\t"
          "s_cbranch_scc1 6b\n\t"
          "s_branch 1b\n\t"
          "5:\n\t"
          "s_cmp_lg_u32 %[cnt], 8\n\t"
          "s_cbranch_scc1 1b\n\t"
          "s_setprio 3\n\t"
          "v_smfmac_f32_32x32x32_bf16 %[ob], %[v11], v[56:63], %[idx]\n\t"
          "s_waitcnt lgkmcnt(0)\n\t"
          "s_nop 11\n\t"
          : [oa] "+v"(oa), [ob] "+v"(ob), [k] "=&v"(kf), [v00] "+v"(v00), [v01] "+v"(v01), [v10] "+v"(v10), [v11] "+v"(v11),
            [ka] "+v"(ka), [va] "+v"(va), [tv] "=&v"(tv), [cnt] "=&s"(cnt), [t0] "=&s"(t0), [t1] "=&s"(t1), [t2] "=&s"(t2)
          : [qa] "v"(qown), [qb] "v"(qoth), [c] "v"(cinit), [idx] "v"(0xddddddddu), [cfull] "v"(C_FULL),
            [msk] "s"(0x0000ffff0000ffffull)
          : "memory", "scc", "v32", "v33", "v34", "v35", "v36", "v37", "v38", "v39", "v40", "v41", "v42", "v43", "v44", "v45", "v46", "v47", "v48", "v49", "v50", "v51", "v52", "v53", "v54", "v55", "v56", "v57", "v58", "v59", "v60", "v61", "v62", "v63");
    }
    STAMP(10);
    f32x4 oe, of;
    float lsum;
    lds_wait<0>(C_CD, 4);
    {
      unsigned fl, sv, left = 64 * SPIN_CAP;
      asm volatile("ds_write_b128 %[xo], %[b0]\n\tds_write_b128 %[xo], %[b1] offset:1024\n\tds_write_b32 %[xl], %[b2]\n\t"
                   "ds_write_b32 %[fx], %[one]\n\ts_mov_b64 exec, 1\n\tds_add_u32 %[od], %[one]\n\ts_mov_b64 exec, -1\n\t"
                   "1:\n\tds_read_b32 %[fl], %[fy]\n\tds_read_b128 %[e], %[yo]\n\tds_read_b128 %[f], %[yo] offset:1024\n\tds_read_b32 %[l], %[yl]\n\t"
                   "s_waitcnt lgkmcnt(0)\n\tv_readfirstlane_b32 %[sv], %[fl]\n\ts_cmp_lg_u32 %[sv], 0\n\ts_cbranch_scc1 2f\n\t"
                   "s_sub_u32 %[left], %[left], 1\n\ts_cbranch_scc0 1b\n\t2:"
                   : [e] "=&v"(oe), [f] "=&v"(of), [l] "=&v"(lsum), [fl] "=&v"(fl), [sv] "=&s"(sv), [left] "+s"(left)
                   : [xo] "v"(xo), [xl] "v"(xl), [yo] "v"(yo), [yl] "v"(yl), [b0] "v"(f32x4{ob[0], ob[1], ob[2], ob[3]}),
                     [b1] "v"(f32x4{ob[4], ob[5], ob[6], ob[7]}), [b2] "v"(ob[8]), [fx] "v"(F_AP + wave * 4),
                     [fy] "v"(F_AP + (wave ^ 1) * 4), [od] "v"(C_OD), [one] "v"(1u)
                   : "memory", "scc");
      oe += f32x4{oa[0], oa[1], oa[2], oa[3]};
      of += f32x4{oa[4], oa[5], oa[6], oa[7]};
      lsum += oa[8];
    }
    if (!__all(lsum >= 0x1p-40f)) {
      f32x16 o = {};
      float m = -3.0e38f;
#pragma unroll 1
      for (int t = 0; t < 64; ++t) {
        const unsigned va = WS_KV + t * 2048 + 1024 + hh * 256 + l15 * 16;
        const u32x4 kt = buf_ld128_dev(ws, WS_KV + t * 2048 + lane * 16);
        u32x4 v0 = buf_ld128_dev(ws, va), v1 = buf_ld128_dev(ws, va + 512);
        f32x16 s = __builtin_amdgcn_mfma_f32_32x32x16_bf16(as_bf8(kt), as_bf8(qown), f32x16{}, 0, 0, 0);
        float tm = s[0];
#pragma unroll
        for (int i = 1; i < 16; ++i) tm = fmaxf(tm, s[i]);
        tm = fmaxf(tm, __shfl_xor(tm, 32));
        const float mn = fmaxf(m, tm), al = __builtin_amdgcn_exp2f(m - mn);
        m = mn;
        f32x8 pa, pb;
#pragma unroll
        for (int i = 0; i < 8; ++i) { pa[i] = __builtin_amdgcn_exp2f(s[i] - mn); pb[i] = __builtin_amdgcn_exp2f(s[8 + i] - mn); }
        o *= al;
        const u32x4 ones = {0x3f803f80u, 0x3f803f80u, 0x3f803f80u, 0x3f803f80u};
        if (l31 >= 16) { v0 = ones; v1 = ones; }
        o = __builtin_amdgcn_smfmac_f32_32x32x32_bf16(as_bf8(v0), __builtin_bit_cast(bf16x16, pa), o, (int)0xddddddddu, 0, 0);
        o = __builtin_amdgcn_smfmac_f32_32x32x32_bf16(as_bf8(v1), __builtin_bit_cast(bf16x16, pb), o, (int)0xddddddddu, 0, 0);
      }
      oe = f32x4{o[0], o[1], o[2], o[3]};
      of = f32x4{o[4], o[5], o[6], o[7]};
      lsum = o[8];
    }
    const float inv = __builtin_amdgcn_rcpf(lsum);
    oe *= inv;
    of *= inv;
    buf_st128_dev(ws, oo, 0, u32x4{pack2(oe[0], oe[1]), pack2(oe[2], oe[3]), pack2(of[0], of[1]), pack2(of[2], of[3])});
    STAMP(8);
    publish_join(ws, L_MISC + 116, 8, WS_FO + (qb * 8 + hg) * 128, epoch << 16, lane);
    STAMP(11);
    STAMP_DUMP();
    return;
  }

  const int w = wave - 8;
  const int dg = xcd, tb = j;
  const int cl = w >> 1, kh = w & 1, ch = 64 * dg + 2 * j + cl;
  const int c2 = lane & 1, g2 = lane >> 1, cc = g4 & 1, th = g4 >> 1, r8 = lane >> 3, c8 = lane & 7;
  const unsigned tl = L_T + w * T_WAVE, wol = (w & 1) * 16384 + (w >> 1) * 65536;
  unsigned kvo, ko0, ko1, ko2, kra, tra, twa, twb, tua, uso, ulo, uw0, uw1, zw, aa, ua, va, vb, vz, exw, exr, urd, uwr, zr0, zr1, zso, qlo, zlo, ztw, ztr, gso, wa, wo0, l16, aso;
  auto addresses = [&]() {
    kvo = (64 * tb + 2 * g2) * 1024 + (64 * dg + 16 * w + 8 * c2) * 2;
    ko0 = 64 * tb - 3 + g2 < 0 ? OOB : kvo - (g2 + 3) * 1024; ko1 = kvo + (29 - g2) * 1024; ko2 = kvo + (61 - g2) * 1024;
    kra = tl + 2 * g2 * 32 + c2 * 16; tra = tl + (31 - 8 * g4 - tq) * 32 + 8 * tp - 128;
    twa = tl + l15 * 128 + ((g4 ^ (l15 & 7)) << 4); twb = twa ^ 64; tua = tl + r8 * 128 + ((c8 ^ r8) << 4);
    uso = WS_U + (64 * dg + 16 * w + r8) * 4096 + (31 - tb) * 128 + c8 * 16;
    ulo = WS_U + ch * 4096 + (kh * 128 + lane) * 16;
    uw0 = L_U + (cl ? 94 - 16 * kh - r8 : 16 * kh + r8) * U_ROW + c8 * 16; uw1 = uw0 + (cl ? -8 : 8) * (int)U_ROW;
    zw = L_U + 32 * U_ROW + 16 * (w * 64 + lane);
    aa = L_A + cl * A_CH + (l31 & 1) * A_COPY + 4 * (4 * hh + (l31 >> 1)) + 1536 * kh;
    ua = L_U + (cl ? 90 - l31 - 12 * kh : l31 + 12 * kh) * U_ROW + 16 * hh;
    vb = L_U + (cl ? 73 - l15 : l15 + 16) * U_ROW + 16 * hh;
    va = vb + ((g4 & 1) ? (cl ? 208 : -80) : 0);
    vz = (g4 & 1) ? L_U + 40 * U_ROW : va;
    vb -= (g4 & 1) * 64;
    exw = L_A + cl * A_CH + lane * 16 + kh * 4096; exr = exw ^ 4096;
    urd = L_U + (cl ? 94 - l31 : l31) * U_ROW + 64 - 64 * kh + 8 - 8 * hh; uwr = urd - 8 + 16 * hh;
    zr0 = uw0 - c8 * 16 + ((c8 ^ 4) << 4); zr1 = uw1 - c8 * 16 + ((c8 ^ 4) << 4);
    zso = WS_Z + ((dg * 32 + 23 - 16 * kh - r8) * 64 + 2 * j + cl) * 128 + c8 * 16;
    qlo = (64 * tb + 16 * th + l15) * 1024 + (64 * dg + 16 * w + 8 * cc) * 2;
    zlo = WS_Z + ((dg * 32 + tb) * 64 + 16 * w) * 128 + lane * 16;
    ztw = L_ZT + w * 2048 + lane * 16; ztr = L_ZT + w * 2048 + (8 * cc + tq) * 128 + (16 * th + 4 * tp) * 2;
    gso = (64 * tb + 16 * th + l15) * 2048 + 1024 + (64 * dg + 16 * w + 8 * cc) * 2;
    wa = wol + (4 * hh + tq) * 64 + (g4 & 1) * 32 + tp * 8;
    wo0 = (lane >> 2) * 1024 + (lane & 3) * 16; l16 = lane * 16;
    aso = (64 * qb + 32 * (hg & 1) + l31) * 2048 + (128 * (hg >> 1) + 32 * w) * 2 + 16 * hh;
    pin(kvo); pin(ko0); pin(ko1); pin(ko2); pin(kra); pin(tra); pin(twa); pin(twb); pin(tua); pin(uso); pin(ulo); pin(uw0); pin(uw1);
    pin(zw); pin(aa); pin(ua); pin(va); pin(vb); pin(vz); pin(exw); pin(exr); pin(urd); pin(uwr); pin(zr0); pin(zr1); pin(zso); pin(qlo); pin(zlo); pin(ztw);
    pin(ztr); pin(gso); pin(wa); pin(wo0); pin(l16); pin(aso);
  };
  u64 epoch = 0;
  __builtin_amdgcn_s_setprio(3);
  if (w == 0) {
#pragma unroll
    for (int i = 0; i < 4; ++i) dma16x4((const char*)(isv ? p.wv : p.wk) + i * 4096, lane * 16, L_WP + i * 4096);
#pragma unroll
    for (int i = 0; i < 16; ++i) dma16((const char*)p.x + (qb * 64 + crit + i) * 1024, (lane ^ i) << 4, L_X + (crit + i) * 1024);
    barrier();
    u32x4 xf[16], wf[16];
    unsigned xa[4];
#pragma unroll
    for (int i = 0; i < 4; ++i) {
      xa[i] = L_X + (crit + l15) * 1024 + (((4 * i + g4) ^ l15) << 4);
      pin(xa[i]);
    }
    unsigned wa = L_WP + (8 * g4 + tq) * 32 + 8 * tp;
    const unsigned tile = WS_KV + (2 * qb + (hg & 1)) * 2048 + (g4 & 1) * 8;
    unsigned ko = isv ? tile + 1024 + blk * 512 + (g4 >> 1) * 256 + l15 * 16 : tile + (g4 >> 1) * 512 + (16 * blk + l15) * 16;
    pin(wa);
    pin(ko);
    asm volatile("s_load_dwordx2 %0, %1, 0x0\n\ts_waitcnt vmcnt(16) lgkmcnt(0)" : "=s"(epoch) : "s"(p.ws) : "memory");
    epoch = (epoch + 1) & 0xffffffffffffull;
    STAMP(1);
#pragma unroll
    for (int i = 0; i < 16; ++i) wf[i] = lds_tr2(wa + i * 1024, 128);
    wait_vm0();
#pragma unroll
    for (int i = 0; i < 16; ++i) xf[i] = lds_rd128(xa[i & 3] + (i >> 2) * 256);
    fence_sched();
    f32x4 d = {};
    if (isv) {
#pragma unroll
      for (int i = 0; i < 16; ++i) d = __builtin_amdgcn_mfma_f32_16x16x32_bf16(as_bf8(xf[i]), as_bf8(wf[i]), d, 0, 0, 0);
    } else {
#pragma unroll
      for (int i = 0; i < 16; ++i) d = __builtin_amdgcn_mfma_f32_16x16x32_bf16(as_bf8(wf[i]), as_bf8(xf[i]), d, 0, 0, 0);
    }
    const u32x2 r = {pack2(d[0], d[1]), pack2(d[2], d[3])};
    buf_st64_dev(ws, ko, r);
    const float nk = sum_halves(sum_rows(bf_lo(r.x) * bf_lo(r.x) + bf_hi(r.x) * bf_hi(r.x) + bf_lo(r.y) * bf_lo(r.y) + bf_hi(r.y) * bf_hi(r.y)));
    unsigned kb = max_in_row((__float_as_uint(__builtin_amdgcn_sqrtf(nk)) >> 16) + 1u);
    kb = isv ? 0u : min(kb, 0x7f80u);
    pin(kb);
    STAMP(12);
    wait_vm0();
    if (lane < 8) store_flag(ws, WS_FA + lane * 8192 + b * 32, (epoch << 16) | kb);
    if (lane == 0) *(LAS u32x2*)(uintptr_t)W_EPOCH = u32x2{(unsigned)epoch, (unsigned)(epoch >> 32)};
    STAMP(13);
    barrier();
    asm volatile("s_barrier" ::: "memory");
    unsigned km = max_in_row(wait_flags<4, 2>(ws, WS_FA + xcd * 8192 + lane * 32, 2048, epoch));
    auto r16 = __builtin_amdgcn_permlane16_swap(km, km, false, false);
    km = max(r16[0], r16[1]);
    auto r32 = __builtin_amdgcn_permlane32_swap(km, km, false, false);
    km = max(r32[0], r32[1]);
    if (lane == 0) *(LAS unsigned*)(uintptr_t)W_KMAX = km << 16;
    STAMP(14);
    barrier();
    addresses();
  } else {
    barrier();
    addresses();
    barrier();
    if (w == 1 && lane < 32) dma16(p.shk, 64 * dg * 8 + lane * 16, L_SH);
    const rsrc_t hr = mk_rsrc((const char*)p.ssk + (64 * dg + 2 * j) * 4096, 8192);
    u32x4 he[3];
    unsigned hp[3];
#pragma unroll
    for (int r = 0; r < 3; ++r) {
      const int n = (w - 1) * 64 + lane + 192 * r, c = n >= 264, hc = n - 264 * c;
      he[r] = buf_ld128(hr, hc < 7 || n >= 528 ? OOB : c * 4096 + (hc == 7 ? 0 : 16 * hc - 124));
      const int n0 = (w - 1) * 64 + 192 * r, c0 = n0 >= 264, h0 = n0 - 264 * c0;
      hp[r] = h0 < 8 ? 0u : ((const CAS unsigned*)p.ssk)[(64 * dg + 2 * j + c0) * 1024 + 4 * h0 - 32];
    }
    fence_sched();
    asm volatile("s_barrier" ::: "memory");
#pragma unroll
    for (int r = 0; r < 3; ++r) {
      const int n = (w - 1) * 64 + lane + 192 * r, c = n >= 264, hc = n - 264 * c;
      u32x4 e = he[r];
      if (hc == 7) e = u32x4{0u, 0u, 0u, e.x};
      unsigned pv = __builtin_amdgcn_update_dpp(hp[r], e.w, 0x138, 0xf, 0xf, false);
      if (hc == 0) pv = 0;
      const u32x4 o = {__builtin_amdgcn_alignbit(e.x, pv, 16), __builtin_amdgcn_alignbit(e.y, e.x, 16),
                       __builtin_amdgcn_alignbit(e.z, e.y, 16), __builtin_amdgcn_alignbit(e.w, e.z, 16)};
      if (r < 2 || n < 528) {
        lds_wr128(L_A + c * A_CH + 16 * hc, o);
        lds_wr128(L_A + c * A_CH + A_COPY + 16 * hc, e);
      }
    }
    wait_vm0();
    barrier();
  }
  STAMP(2);
  f32x2 sw[16];
#pragma unroll
  for (int e = 0; e < 4; ++e) {
    const u32x4 sr = lds_rd128(L_SH + w * 128 + c2 * 64 + e * 16);
    sw[4 * e] = f32x2{bf_lo(sr.x), bf_lo(sr.z)};
    sw[4 * e + 1] = f32x2{bf_hi(sr.x), bf_hi(sr.z)};
    sw[4 * e + 2] = f32x2{bf_lo(sr.y), bf_lo(sr.w)};
    sw[4 * e + 3] = f32x2{bf_hi(sr.y), bf_hi(sr.w)};
  }
#pragma unroll
  for (int i = 0; i < 16; ++i) pin(sw[i]);
  epoch = lds_epoch();
  fence_sched();
  const float dd = bf_lo(((const CAS unsigned*)p.ssd)[ch >> 1] >> (16 * (ch & 1)));
  asm volatile("s_barrier" ::: "memory");
  STAMP(3);
  if (b == 0 && tid == 512) store_flag(ws, WS_EPOCH, epoch);
  auto stage_kv = [&](int i) {
    const unsigned off = 16384 + (i & 1) * 65536 + ((i >> 1) * 4 + w) * 4096;
    dma16x4_dev((const char*)p.ws + WS_KV + off, l16, L_KV + off);
  };
  stage_kv(0);
  stage_kv(1);
  {
    const rsrc_t kk = mk_rsrc(p.bk, 2048 * 1024), vv = mk_rsrc(p.bv, 2048 * 1024);
    dma16_buf(kk, ko0, tl);
    dma16_buf(kk, ko1, tl + 1024);
    if (lane < 6) dma16_buf(kk, ko2, tl + 2048);
    u32x4 vr[2], kr[5], kv[2];
    asm volatile("buffer_load_dwordx4 %0, %2, %3, 0 offen\n\tbuffer_load_dwordx4 %1, %2, %3, 0 offen offset:1024\n\ts_waitcnt vmcnt(5)"
                 : "=&v"(vr[0]), "=&v"(vr[1]) : "v"(kvo), "s"(vv) : "memory");
    lds_signal(C_FULL, lane);
    STAMP(4);
#pragma unroll
    for (int i = 2; i < 6; ++i) stage_kv(i);
    asm volatile("s_waitcnt vmcnt(16)" : "+v"(vr[0]), "+v"(vr[1]) :: "memory");
#pragma unroll
    for (int i = 0; i < 5; ++i) kr[i] = lds_rd128(kra + i * 32);
#pragma unroll
    for (int e = 0; e < 4; ++e) {
      f32x2 kf[5];
#pragma unroll
      for (int i = 0; i < 5; ++i) kf[i] = f32x2{bf_lo(kr[i][e]), bf_hi(kr[i][e])};
#pragma unroll
      for (int r = 0; r < 2; ++r) {
        const f32x2 ks = (kf[r] * sw[4 * e + 3] + kf[r + 1] * sw[4 * e + 2] + kf[r + 2] * sw[4 * e + 1] + kf[r + 3] * sw[4 * e]) *
                         f32x2{bf_lo(vr[r][e]), bf_hi(vr[r][e])};
        kv[r][e] = pack2(ks.x, ks.y);
      }
    }
    lds_wr128(kra, kv[0]);
    lds_wr128(kra + 32, kv[1]);
    wait_lgkm0();
    {
      const u32x2 a0 = lds_tr(tra + 1152), b0 = lds_tr(tra + 1024), a1 = lds_tr(tra + 128), b1 = lds_tr(tra);
      kv[0] = u32x4{a0.x, a0.y, b0.x, b0.y};
      kv[1] = u32x4{a1.x, a1.y, b1.x, b1.y};
    }
    wait_lgkm0();
    lds_wr128(twa, kv[0]);
    lds_wr128(twb, kv[1]);
    wait_lgkm0();
    kv[0] = lds_rd128(tua);
    kv[1] = lds_rd128(tua + 1024);
    buf_st128_dev(ws, uso, 0, kv[0]);
    buf_st128_dev(ws, uso, 32768, kv[1]);
    lds_signal(C_B1, lane);
    STAMP(5);
    asm volatile("s_waitcnt vmcnt(2)" ::: "memory");
    lds_signal(C_FULL2, lane);
    wait_vm0();
    if (lane == 0) store_flag(ws, WS_FB + ((dg * 4 + w) * 32 + tb) * 128, epoch << 16);
    STAMP(6);
    lds_wait<1>(C_B1, 4);
  }
  {
    lds_wr128(zw, u32x4{0, 0, 0, 0});
    if (w == 0) {
      if (lane < 23) lds_wr128(zw + 4096, u32x4{0, 0, 0, 0});
      wait_flags<1, 4>(ws, WS_FB + ((dg * 4 + (j >> 3)) * 32 + l31) * 128, 0, epoch);
      lds_signal(C_FB, lane);
    } else {
      lds_wait<2>(C_FB, 1);
    }
    const u32x4 u0 = buf_ld128_dev(ws, ulo), u1 = buf_ld128_dev(ws, ulo + 1024);
    lds_wr128(uw0, u0);
    lds_wr128(uw1, u1);
  }
  lds_signal(C_CS, lane);
  lds_wait<1>(C_CS, 4);
  STAMP(7);
  u32x4 qv[2], wf[16];
  const char* wob = (const char*)p.wo + (hg >> 1) * 256 + w * 64;
  {
    f32x16 y0 = {}, y1 = {}, y2 = {};
    {
      u32x4 u0, u1, u2, u3;
      unsigned cnt = kh ? 6 : 11, t0, t1, tv;
      unsigned wp0 = wo0, wp1 = wp0 + 15 * 1024, wp2 = wp0 + 30 * 1024, wp3 = wp0 + 45 * 1024;
      const unsigned wi0 = 65536, wi1 = 65536, wi2 = 65536;
      if (cl == 0) {
        asm volatile(
            "ds_read2_b32 v[32:33], %[aa] offset0:0 offset1:1\n\t"
            "ds_read2_b32 v[34:35], %[aa] offset0:2 offset1:3\n\t"
            "ds_read2_b32 v[36:37], %[aa] offset0:8 offset1:9\n\t"
            "ds_read2_b32 v[38:39], %[aa] offset0:10 offset1:11\n\t"
            "ds_read2_b32 v[40:41], %[aa] offset0:16 offset1:17\n\t"
            "ds_read2_b32 v[42:43], %[aa] offset0:18 offset1:19\n\t"
            "ds_read_b128 %[u0], %[ua] offset:0\n\t"
            "ds_read2_b32 v[44:45], %[aa] offset0:24 offset1:25\n\t"
            "ds_read2_b32 v[46:47], %[aa] offset0:26 offset1:27\n\t"
            "ds_read_b128 %[u1], %[ua] offset:32\n\t"
            "1:\n\t"
            "s_waitcnt lgkmcnt(3)\n\t"
            "v_mfma_f32_32x32x16_bf16 %[y0], v[32:35], %[u0], %[y0]\n\t"
            "s_sleep 1\n\t"
            "v_mfma_f32_32x32x16_bf16 %[y1], v[40:43], %[u0], %[y1]\n\t"
            "ds_read2_b32 v[32:33], %[aa] offset0:32 offset1:33\n\t"
            "ds_read2_b32 v[34:35], %[aa] offset0:34 offset1:35\n\t"
            "ds_read_b128 %[u0], %[ua] offset:64\n\t"
            "s_sleep 1\n\t"
            "s_waitcnt lgkmcnt(3)\n\t"
            "v_mfma_f32_32x32x16_bf16 %[y0], v[36:39], %[u1], %[y0]\n\t"
            "s_sleep 1\n\t"
            "v_mfma_f32_32x32x16_bf16 %[y1], v[44:47], %[u1], %[y1]\n\t"
            "ds_read2_b32 v[36:37], %[aa] offset0:40 offset1:41\n\t"
            "ds_read2_b32 v[38:39], %[aa] offset0:42 offset1:43\n\t"
            "ds_read_b128 %[u1], %[ua] offset:96\n\t"
            "s_sleep 1\n\t"
            "s_waitcnt lgkmcnt(3)\n\t"
            "v_mfma_f32_32x32x16_bf16 %[y0], v[40:43], %[u0], %[y0]\n\t"
            "s_sleep 1\n\t"
            "v_mfma_f32_32x32x16_bf16 %[y1], v[32:35], %[u0], %[y1]\n\t"
            "ds_read2_b32 v[40:41], %[aa] offset0:48 offset1:49\n\t"
            "ds_read2_b32 v[42:43], %[aa] offset0:50 offset1:51\n\t"
            "ds_read_b128 %[u0], %[ua] offset:144\n\t"
            "s_sleep 1\n\t"
            "s_waitcnt lgkmcnt(3)\n\t"
            "v_mfma_f32_32x32x16_bf16 %[y0], v[44:47], %[u1], %[y0]\n\t"
            "s_sleep 1\n\t"
            "v_mfma_f32_32x32x16_bf16 %[y1], v[36:39], %[u1], %[y1]\n\t"
            "ds_read2_b32 v[44:45], %[aa] offset0:56 offset1:57\n\t"
            "ds_read2_b32 v[46:47], %[aa] offset0:58 offset1:59\n\t"
            "ds_read_b128 %[u1], %[ua] offset:176\n\t"
            "s_sleep 1\n\t"
            "s_waitcnt lgkmcnt(3)\n\t"
            "v_mfma_f32_32x32x16_bf16 %[y0], v[32:35], %[u0], %[y0]\n\t"
            "s_sleep 1\n\t"
            "v_mfma_f32_32x32x16_bf16 %[y1], v[40:43], %[u0], %[y1]\n\t"
            "ds_read2_b32 v[32:33], %[aa] offset0:64 offset1:65\n\t"
            "ds_read2_b32 v[34:35], %[aa] offset0:66 offset1:67\n\t"
            "ds_read_b128 %[u0], %[ua] offset:208\n\t"
            "s_sleep 1\n\t"
            "s_waitcnt lgkmcnt(3)\n\t"
            "v_mfma_f32_32x32x16_bf16 %[y0], v[36:39], %[u1], %[y0]\n\t"
            "s_sleep 1\n\t"
            "v_mfma_f32_32x32x16_bf16 %[y1], v[44:47], %[u1], %[y1]\n\t"
            "ds_read2_b32 v[36:37], %[aa] offset0:72 offset1:73\n\t"
            "ds_read2_b32 v[38:39], %[aa] offset0:74 offset1:75\n\t"
            "ds_read_b128 %[u1], %[ua] offset:240\n\t"
            "s_sleep 1\n\t"
            "s_waitcnt lgkmcnt(3)\n\t"
            "v_mfma_f32_32x32x16_bf16 %[y0], v[40:43], %[u0], %[y0]\n\t"
            "s_sleep 1\n\t"
            "v_mfma_f32_32x32x16_bf16 %[y1], v[32:35], %[u0], %[y1]\n\t"
            "ds_read2_b32 v[40:41], %[aa] offset0:80 offset1:81\n\t"
            "ds_read2_b32 v[42:43], %[aa] offset0:82 offset1:83\n\t"
            "ds_read_b128 %[u0], %[ua] offset:288\n\t"
            "s_sleep 1\n\t"
            "s_waitcnt lgkmcnt(3)\n\t"
            "v_mfma_f32_32x32x16_bf16 %[y0], v[44:47], %[u1], %[y0]\n\t"
            "s_sleep 1\n\t"
            "v_mfma_f32_32x32x16_bf16 %[y1], v[36:39], %[u1], %[y1]\n\t"
            "ds_read2_b32 v[44:45], %[aa] offset0:88 offset1:89\n\t"
            "ds_read2_b32 v[46:47], %[aa] offset0:90 offset1:91\n\t"
            "ds_read_b128 %[u1], %[ua] offset:320\n\t"
            "s_sleep 1\n\t"
            "s_waitcnt lgkmcnt(3)\n\t"
            "v_mfma_f32_32x32x16_bf16 %[y0], v[32:35], %[u0], %[y0]\n\t"
            "s_sleep 1\n\t"
            "v_mfma_f32_32x32x16_bf16 %[y1], v[40:43], %[u0], %[y1]\n\t"
            "ds_read2_b32 v[32:33], %[aa] offset0:96 offset1:97\n\t"
            "ds_read2_b32 v[34:35], %[aa] offset0:98 offset1:99\n\t"
            "ds_read_b128 %[u0], %[ua] offset:352\n\t"
            "s_sleep 1\n\t"
            "s_waitcnt lgkmcnt(3)\n\t"
            "v_mfma_f32_32x32x16_bf16 %[y0], v[36:39], %[u1], %[y0]\n\t"
            "s_sleep 1\n\t"
            "v_mfma_f32_32x32x16_bf16 %[y1], v[44:47], %[u1], %[y1]\n\t"
            "ds_read2_b32 v[36:37], %[aa] offset0:104 offset1:105\n\t"
            "ds_read2_b32 v[38:39], %[aa] offset0:106 offset1:107\n\t"
            "ds_read_b128 %[u1], %[ua] offset:384\n\t"
            "s_sleep 1\n\t"
            "s_waitcnt lgkmcnt(3)\n\t"
            "v_mfma_f32_32x32x16_bf16 %[y0], v[40:43], %[u0], %[y0]\n\t"
            "s_sleep 1\n\t"
            "v_mfma_f32_32x32x16_bf16 %[y1], v[32:35], %[u0], %[y1]\n\t"
            "ds_read2_b32 v[40:41], %[aa] offset0:112 offset1:113\n\t"
            "ds_read2_b32 v[42:43], %[aa] offset0:114 offset1:115\n\t"
            "ds_read_b128 %[u0], %[ua] offset:432\n\t"
            "s_sleep 1\n\t"
            "s_waitcnt lgkmcnt(3)\n\t"
            "v_mfma_f32_32x32x16_bf16 %[y0], v[44:47], %[u1], %[y0]\n\t"
            "s_sleep 1\n\t"
            "v_mfma_f32_32x32x16_bf16 %[y1], v[36:39], %[u1], %[y1]\n\t"
            "ds_read2_b32 v[44:45], %[aa] offset0:120 offset1:121\n\t"
            "ds_read2_b32 v[46:47], %[aa] offset0:122 offset1:123\n\t"
            "ds_read_b128 %[u1], %[ua] offset:464\n\t"
            "s_sleep 1\n\t"
            "s_waitcnt lgkmcnt(3)\n\t"
            "v_mfma_f32_32x32x16_bf16 %[y0], v[32:35], %[u0], %[y0]\n\t"
            "s_sleep 1\n\t"
            "v_mfma_f32_32x32x16_bf16 %[y1], v[40:43], %[u0], %[y1]\n\t"
            "ds_read2_b32 v[32:33], %[aa] offset0:128 offset1:129\n\t"
            "ds_read2_b32 v[34:35], %[aa] offset0:130 offset1:131\n\t"
            "ds_read_b128 %[u0], %[ua] offset:496\n\t"
            "s_sleep 1\n\t"
            "s_waitcnt lgkmcnt(3)\n\t"
            "v_mfma_f32_32x32x16_bf16 %[y0], v[36:39], %[u1], %[y0]\n\t"
            "s_sleep 1\n\t"
            "v_mfma_f32_32x32x16_bf16 %[y1], v[44:47], %[u1], %[y1]\n\t"
            "ds_read2_b32 v[36:37], %[aa] offset0:136 offset1:137\n\t"
            "ds_read2_b32 v[38:39], %[aa] offset0:138 offset1:139\n\t"
            "ds_read_b128 %[u1], %[ua] offset:528\n\t"
            "s_sleep 1\n\t"
            "s_waitcnt lgkmcnt(3)\n\t"
            "v_mfma_f32_32x32x16_bf16 %[y0], v[40:43], %[u0], %[y0]\n\t"
            "s_sleep 1\n\t"
            "v_mfma_f32_32x32x16_bf16 %[y1], v[32:35], %[u0], %[y1]\n\t"
            "ds_read2_b32 v[40:41], %[aa] offset0:144 offset1:145\n\t"
            "ds_read2_b32 v[42:43], %[aa] offset0:146 offset1:147\n\t"
            "ds_read_b128 %[u0], %[ua] offset:576\n\t"
            "s_sleep 1\n\t"
            "s_waitcnt lgkmcnt(3)\n\t"
            "v_mfma_f32_32x32x16_bf16 %[y0], v[44:47], %[u1], %[y0]\n\t"
            "s_sleep 1\n\t"
            "v_mfma_f32_32x32x16_bf16 %[y1], v[36:39], %[u1], %[y1]\n\t"
            "ds_read2_b32 v[44:45], %[aa] offset0:152 offset1:153\n\t"
            "ds_read2_b32 v[46:47], %[aa] offset0:154 offset1:155\n\t"
            "ds_read_b128 %[u1], %[ua] offset:608\n\t"
            "s_sleep 1\n\t"
            "v_add_u32 %[aa], 512, %[aa]\n\t"
            "v_add_u32 %[ua], 576, %[ua]\n\t"
            "s_sub_u32 %[cnt], %[cnt], 1\n\t"
            "s_cmp_eq_u32 %[cnt], 5\n\t"
            "s_cbranch_scc1 5f\n\t"
            "s_cmp_eq_u32 %[cnt], 8\n\t"
            "s_cbranch_scc1 9f\n\t"
            "s_cmp_lg_u32 %[cnt], 9\n\t"
            "s_cbranch_scc1 1b\n\t"
            "7:\n\t"
            "s_mov_b32 %[t0], 0x200000\n\t"
            "2:\n\t"
            "ds_read_b32 %[tv], %[chalf]\n\t"
            "s_waitcnt lgkmcnt(0)\n\t"
            "v_readfirstlane_b32 %[t1], %[tv]\n\t"
            "s_cmp_ge_u32 %[t1], 8\n\t"
            "s_cbranch_scc1 3f\n\t"
            "s_sleep 4\n\t"
            "s_sub_u32 %[t0], %[t0], 1\n\t"
            "s_cbranch_scc0 2b\n\t"
            "3:\n\t"
            "s_mov_b32 %[t0], m0\n\t"
            "s_mov_b32 m0, %[wl]\n\t"
            "s_nop 0\n\t"
            "global_load_lds_dwordx4 %[wo0], %[wb] offset:0\n\t"
            "global_load_lds_dwordx4 %[wo1], %[wb] offset:1024\n\t"
            "global_load_lds_dwordx4 %[wo2], %[wb] offset:2048\n\t"
            "global_load_lds_dwordx4 %[wo3], %[wb] offset:3072\n\t"
            "s_add_u32 m0, m0, 4096\n\t"
            "v_add_u32 %[wo0], %[wi0], %[wo0]\n\t"
            "v_add_u32 %[wo1], %[wi0], %[wo1]\n\t"
            "v_add_u32 %[wo2], %[wi0], %[wo2]\n\t"
            "v_add_u32 %[wo3], %[wi0], %[wo3]\n\t"
            "s_nop 0\n\t"
            "global_load_lds_dwordx4 %[wo0], %[wb] offset:0\n\t"
            "global_load_lds_dwordx4 %[wo1], %[wb] offset:1024\n\t"
            "global_load_lds_dwordx4 %[wo2], %[wb] offset:2048\n\t"
            "global_load_lds_dwordx4 %[wo3], %[wb] offset:3072\n\t"
            "s_add_u32 m0, m0, 4096\n\t"
            "v_add_u32 %[wo0], %[wi1], %[wo0]\n\t"
            "v_add_u32 %[wo1], %[wi1], %[wo1]\n\t"
            "v_add_u32 %[wo2], %[wi1], %[wo2]\n\t"
            "v_add_u32 %[wo3], %[wi1], %[wo3]\n\t"
            "s_nop 0\n\t"
            "global_load_lds_dwordx4 %[wo0], %[wb] offset:0\n\t"
            "global_load_lds_dwordx4 %[wo1], %[wb] offset:1024\n\t"
            "global_load_lds_dwordx4 %[wo2], %[wb] offset:2048\n\t"
            "global_load_lds_dwordx4 %[wo3], %[wb] offset:3072\n\t"
            "s_add_u32 m0, m0, 4096\n\t"
            "v_add_u32 %[wo0], %[wi2], %[wo0]\n\t"
            "v_add_u32 %[wo1], %[wi2], %[wo1]\n\t"
            "v_add_u32 %[wo2], %[wi2], %[wo2]\n\t"
            "v_add_u32 %[wo3], %[wi2], %[wo3]\n\t"
            "s_nop 0\n\t"
            "global_load_lds_dwordx4 %[wo0], %[wb] offset:0\n\t"
            "global_load_lds_dwordx4 %[wo1], %[wb] offset:1024\n\t"
            "global_load_lds_dwordx4 %[wo2], %[wb] offset:2048\n\t"
            "global_load_lds_dwordx4 %[wo3], %[wb] offset:3072\n\t"
            "s_mov_b32 m0, %[t0]\n\t"
            "s_cmp_eq_u32 %[cnt], 9\n\t"
            "s_cbranch_scc1 1b\n\t"
            "s_branch 6f\n\t"
            "5:\n\t"
            "s_waitcnt lgkmcnt(0)\n\t"
            "ds_read_b128 %[u0], %[vz] offset:0\n\t"
            "ds_read_b128 %[u1], %[vz] offset:32\n\t"
            "ds_read_b128 %[u2], %[vb] offset:64\n\t"
            "ds_read_b128 %[u3], %[vb] offset:96\n\t"
            "s_waitcnt lgkmcnt(0)\n\t"
            "v_mfma_f32_32x32x16_bf16 %[y2], v[32:35], %[u0], %[y2]\n\t"
            "ds_read2_b32 v[32:33], %[aa] offset0:32 offset1:33\n\t"
            "ds_read2_b32 v[34:35], %[aa] offset0:34 offset1:35\n\t"
            "ds_read_b128 %[u0], %[va] offset:144\n\t"
            "s_sleep 1\n\t"
            "v_mfma_f32_32x32x16_bf16 %[y2], v[36:39], %[u1], %[y2]\n\t"
            "ds_read2_b32 v[36:37], %[aa] offset0:40 offset1:41\n\t"
            "ds_read2_b32 v[38:39], %[aa] offset0:42 offset1:43\n\t"
            "ds_read_b128 %[u1], %[va] offset:176\n\t"
            "s_sleep 1\n\t"
            "s_sub_u32 %[cnt], %[cnt], 1\n\t"
            "s_waitcnt lgkmcnt(0)\n\t"
            "6:\n\t"
            "s_waitcnt lgkmcnt(9)\n\t"
            "v_mfma_f32_32x32x16_bf16 %[y2], v[40:43], %[u2], %[y2]\n\t"
            "ds_read2_b32 v[40:41], %[aa] offset0:48 offset1:49\n\t"
            "ds_read2_b32 v[42:43], %[aa] offset0:50 offset1:51\n\t"
            "ds_read_b128 %[u2], %[vb] offset:208\n\t"
            "s_sleep 1\n\t"
            "s_waitcnt lgkmcnt(9)\n\t"
            "v_mfma_f32_32x32x16_bf16 %[y2], v[44:47], %[u3], %[y2]\n\t"
            "ds_read2_b32 v[44:45], %[aa] offset0:56 offset1:57\n\t"
            "ds_read2_b32 v[46:47], %[aa] offset0:58 offset1:59\n\t"
            "ds_read_b128 %[u3], %[vb] offset:240\n\t"
            "s_sleep 1\n\t"
            "s_waitcnt lgkmcnt(9)\n\t"
            "v_mfma_f32_32x32x16_bf16 %[y2], v[32:35], %[u0], %[y2]\n\t"
            "ds_read2_b32 v[32:33], %[aa] offset0:64 offset1:65\n\t"
            "ds_read2_b32 v[34:35], %[aa] offset0:66 offset1:67\n\t"
            "ds_read_b128 %[u0], %[va] offset:288\n\t"
            "s_sleep 1\n\t"
            "s_waitcnt lgkmcnt(9)\n\t"
            "v_mfma_f32_32x32x16_bf16 %[y2], v[36:39], %[u1], %[y2]\n\t"
            "ds_read2_b32 v[36:37], %[aa] offset0:72 offset1:73\n\t"
            "ds_read2_b32 v[38:39], %[aa] offset0:74 offset1:75\n\t"
            "ds_read_b128 %[u1], %[va] offset:320\n\t"
            "s_sleep 1\n\t"
            "s_waitcnt lgkmcnt(9)\n\t"
            "v_mfma_f32_32x32x16_bf16 %[y2], v[40:43], %[u2], %[y2]\n\t"
            "ds_read2_b32 v[40:41], %[aa] offset0:80 offset1:81\n\t"
            "ds_read2_b32 v[42:43], %[aa] offset0:82 offset1:83\n\t"
            "ds_read_b128 %[u2], %[vb] offset:352\n\t"
            "s_sleep 1\n\t"
            "s_waitcnt lgkmcnt(9)\n\t"
            "v_mfma_f32_32x32x16_bf16 %[y2], v[44:47], %[u3], %[y2]\n\t"
            "ds_read2_b32 v[44:45], %[aa] offset0:88 offset1:89\n\t"
            "ds_read2_b32 v[46:47], %[aa] offset0:90 offset1:91\n\t"
            "ds_read_b128 %[u3], %[vb] offset:384\n\t"
            "s_sleep 1\n\t"
            "s_waitcnt lgkmcnt(9)\n\t"
            "v_mfma_f32_32x32x16_bf16 %[y2], v[32:35], %[u0], %[y2]\n\t"
            "ds_read2_b32 v[32:33], %[aa] offset0:96 offset1:97\n\t"
            "ds_read2_b32 v[34:35], %[aa] offset0:98 offset1:99\n\t"
            "ds_read_b128 %[u0], %[va] offset:432\n\t"
            "s_sleep 1\n\t"
            "s_waitcnt lgkmcnt(9)\n\t"
            "v_mfma_f32_32x32x16_bf16 %[y2], v[36:39], %[u1], %[y2]\n\t"
            "ds_read2_b32 v[36:37], %[aa] offset0:104 offset1:105\n\t"
            "ds_read2_b32 v[38:39], %[aa] offset0:106 offset1:107\n\t"
            "ds_read_b128 %[u1], %[va] offset:464\n\t"
            "s_sleep 1\n\t"
            "s_waitcnt lgkmcnt(9)\n\t"
            "v_mfma_f32_32x32x16_bf16 %[y2], v[40:43], %[u2], %[y2]\n\t"
            "ds_read2_b32 v[40:41], %[aa] offset0:112 offset1:113\n\t"
            "ds_read2_b32 v[42:43], %[aa] offset0:114 offset1:115\n\t"
            "ds_read_b128 %[u2], %[vb] offset:496\n\t"
            "s_sleep 1\n\t"
            "s_waitcnt lgkmcnt(9)\n\t"
            "v_mfma_f32_32x32x16_bf16 %[y2], v[44:47], %[u3], %[y2]\n\t"
            "ds_read2_b32 v[44:45], %[aa] offset0:120 offset1:121\n\t"
            "ds_read2_b32 v[46:47], %[aa] offset0:122 offset1:123\n\t"
            "ds_read_b128 %[u3], %[vb] offset:528\n\t"
            "s_sleep 1\n\t"
            "s_waitcnt lgkmcnt(9)\n\t"
            "v_mfma_f32_32x32x16_bf16 %[y2], v[32:35], %[u0], %[y2]\n\t"
            "ds_read2_b32 v[32:33], %[aa] offset0:128 offset1:129\n\t"
            "ds_read2_b32 v[34:35], %[aa] offset0:130 offset1:131\n\t"
            "ds_read_b128 %[u0], %[va] offset:576\n\t"
            "s_sleep 1\n\t"
            "s_waitcnt lgkmcnt(9)\n\t"
            "v_mfma_f32_32x32x16_bf16 %[y2], v[36:39], %[u1], %[y2]\n\t"
            "ds_read2_b32 v[36:37], %[aa] offset0:136 offset1:137\n\t"
            "ds_read2_b32 v[38:39], %[aa] offset0:138 offset1:139\n\t"
            "ds_read_b128 %[u1], %[va] offset:608\n\t"
            "s_sleep 1\n\t"
            "s_waitcnt lgkmcnt(9)\n\t"
            "v_mfma_f32_32x32x16_bf16 %[y2], v[40:43], %[u2], %[y2]\n\t"
            "ds_read2_b32 v[40:41], %[aa] offset0:144 offset1:145\n\t"
            "ds_read2_b32 v[42:43], %[aa] offset0:146 offset1:147\n\t"
            "ds_read_b128 %[u2], %[vb] offset:640\n\t"
            "s_sleep 1\n\t"
            "s_waitcnt lgkmcnt(9)\n\t"
            "v_mfma_f32_32x32x16_bf16 %[y2], v[44:47], %[u3], %[y2]\n\t"
            "ds_read2_b32 v[44:45], %[aa] offset0:152 offset1:153\n\t"
            "ds_read2_b32 v[46:47], %[aa] offset0:154 offset1:155\n\t"
            "ds_read_b128 %[u3], %[vb] offset:672\n\t"
            "s_sleep 1\n\t"
            "s_waitcnt lgkmcnt(9)\n\t"
            "v_mfma_f32_32x32x16_bf16 %[y2], v[32:35], %[u0], %[y2]\n\t"
            "ds_read2_b32 v[32:33], %[aa] offset0:160 offset1:161\n\t"
            "ds_read2_b32 v[34:35], %[aa] offset0:162 offset1:163\n\t"
            "ds_read_b128 %[u0], %[va] offset:720\n\t"
            "s_sleep 1\n\t"
            "s_waitcnt lgkmcnt(9)\n\t"
            "v_mfma_f32_32x32x16_bf16 %[y2], v[36:39], %[u1], %[y2]\n\t"
            "ds_read2_b32 v[36:37], %[aa] offset0:168 offset1:169\n\t"
            "ds_read2_b32 v[38:39], %[aa] offset0:170 offset1:171\n\t"
            "ds_read_b128 %[u1], %[va] offset:752\n\t"
            "s_sleep 1\n\t"
            "v_add_u32 %[aa], 512, %[aa]\n\t"
            "v_add_u32 %[va], 576, %[va]\n\t"
            "v_add_u32 %[vb], 576, %[vb]\n\t"
            "s_sub_u32 %[cnt], %[cnt], 1\n\t"
            "s_cmp_eq_u32 %[cnt], 2\n\t"
            "s_cbranch_scc1 7b\n\t"
            "s_cmp_lg_u32 %[cnt], 0\n\t"
            "s_cbranch_scc1 6b\n\t"
            "9:\n\t"
            "s_waitcnt lgkmcnt(0)\n\t"
            "s_nop 11\n\t"
            : [y0] "+v"(y0), [y1] "+v"(y1), [y2] "+v"(y2), [u0] "=&v"(u0), [u1] "=&v"(u1), [u2] "=&v"(u2), [u3] "=&v"(u3),
              [aa] "+v"(aa), [ua] "+v"(ua), [va] "+v"(va), [vb] "+v"(vb), [cnt] "+s"(cnt), [t0] "=&s"(t0), [t1] "=&s"(t1),
              [tv] "=&v"(tv), [wo0] "+v"(wp0), [wo1] "+v"(wp1), [wo2] "+v"(wp2), [wo3] "+v"(wp3)
            : [vz] "v"(vz), [chalf] "v"(C_HALF), [wb] "s"(wob), [wl] "s"(wol), [wi0] "s"(wi0), [wi1] "s"(wi1), [wi2] "s"(wi2)
            : "memory", "scc", "v32", "v33", "v34", "v35", "v36", "v37", "v38", "v39", "v40", "v41", "v42", "v43", "v44", "v45", "v46", "v47");
      } else {
        asm volatile(
            "ds_read2_b32 v[32:33], %[aa] offset0:0 offset1:1\n\t"
            "ds_read2_b32 v[34:35], %[aa] offset0:2 offset1:3\n\t"
            "ds_read2_b32 v[36:37], %[aa] offset0:8 offset1:9\n\t"
            "ds_read2_b32 v[38:39], %[aa] offset0:10 offset1:11\n\t"
            "ds_read2_b32 v[40:41], %[aa] offset0:16 offset1:17\n\t"
            "ds_read2_b32 v[42:43], %[aa] offset0:18 offset1:19\n\t"
            "ds_read_b128 %[u0], %[ua] offset:576\n\t"
            "ds_read2_b32 v[44:45], %[aa] offset0:24 offset1:25\n\t"
            "ds_read2_b32 v[46:47], %[aa] offset0:26 offset1:27\n\t"
            "ds_read_b128 %[u1], %[ua] offset:608\n\t"
            "1:\n\t"
            "s_waitcnt lgkmcnt(3)\n\t"
            "v_mfma_f32_32x32x16_bf16 %[y0], v[32:35], %[u0], %[y0]\n\t"
            "s_sleep 1\n\t"
            "v_mfma_f32_32x32x16_bf16 %[y1], v[40:43], %[u0], %[y1]\n\t"
            "ds_read2_b32 v[32:33], %[aa] offset0:32 offset1:33\n\t"
            "ds_read2_b32 v[34:35], %[aa] offset0:34 offset1:35\n\t"
            "ds_read_b128 %[u0], %[ua] offset:640\n\t"
            "s_sleep 1\n\t"
            "s_waitcnt lgkmcnt(3)\n\t"
            "v_mfma_f32_32x32x16_bf16 %[y0], v[36:39], %[u1], %[y0]\n\t"
            "s_sleep 1\n\t"
            "v_mfma_f32_32x32x16_bf16 %[y1], v[44:47], %[u1], %[y1]\n\t"
            "ds_read2_b32 v[36:37], %[aa] offset0:40 offset1:41\n\t"
            "ds_read2_b32 v[38:39], %[aa] offset0:42 offset1:43\n\t"
            "ds_read_b128 %[u1], %[ua] offset:672\n\t"
            "s_sleep 1\n\t"
            "s_waitcnt lgkmcnt(3)\n\t"
            "v_mfma_f32_32x32x16_bf16 %[y0], v[40:43], %[u0], %[y0]\n\t"
            "s_sleep 1\n\t"
            "v_mfma_f32_32x32x16_bf16 %[y1], v[32:35], %[u0], %[y1]\n\t"
            "ds_read2_b32 v[40:41], %[aa] offset0:48 offset1:49\n\t"
            "ds_read2_b32 v[42:43], %[aa] offset0:50 offset1:51\n\t"
            "ds_read_b128 %[u0], %[ua] offset:432\n\t"
            "s_sleep 1\n\t"
            "s_waitcnt lgkmcnt(3)\n\t"
            "v_mfma_f32_32x32x16_bf16 %[y0], v[44:47], %[u1], %[y0]\n\t"
            "s_sleep 1\n\t"
            "v_mfma_f32_32x32x16_bf16 %[y1], v[36:39], %[u1], %[y1]\n\t"
            "ds_read2_b32 v[44:45], %[aa] offset0:56 offset1:57\n\t"
            "ds_read2_b32 v[46:47], %[aa] offset0:58 offset1:59\n\t"
            "ds_read_b128 %[u1], %[ua] offset:464\n\t"
            "s_sleep 1\n\t"
            "s_waitcnt lgkmcnt(3)\n\t"
            "v_mfma_f32_32x32x16_bf16 %[y0], v[32:35], %[u0], %[y0]\n\t"
            "s_sleep 1\n\t"
            "v_mfma_f32_32x32x16_bf16 %[y1], v[40:43], %[u0], %[y1]\n\t"
            "ds_read2_b32 v[32:33], %[aa] offset0:64 offset1:65\n\t"
            "ds_read2_b32 v[34:35], %[aa] offset0:66 offset1:67\n\t"
            "ds_read_b128 %[u0], %[ua] offset:496\n\t"
            "s_sleep 1\n\t"
            "s_waitcnt lgkmcnt(3)\n\t"
            "v_mfma_f32_32x32x16_bf16 %[y0], v[36:39], %[u1], %[y0]\n\t"
            "s_sleep 1\n\t"
            "v_mfma_f32_32x32x16_bf16 %[y1], v[44:47], %[u1], %[y1]\n\t"
            "ds_read2_b32 v[36:37], %[aa] offset0:72 offset1:73\n\t"
            "ds_read2_b32 v[38:39], %[aa] offset0:74 offset1:75\n\t"
            "ds_read_b128 %[u1], %[ua] offset:528\n\t"
            "s_sleep 1\n\t"
            "s_waitcnt lgkmcnt(3)\n\t"
            "v_mfma_f32_32x32x16_bf16 %[y0], v[40:43], %[u0], %[y0]\n\t"
            "s_sleep 1\n\t"
            "v_mfma_f32_32x32x16_bf16 %[y1], v[32:35], %[u0], %[y1]\n\t"
            "ds_read2_b32 v[40:41], %[aa] offset0:80 offset1:81\n\t"
            "ds_read2_b32 v[42:43], %[aa] offset0:82 offset1:83\n\t"
            "ds_read_b128 %[u0], %[ua] offset:288\n\t"
            "s_sleep 1\n\t"
            "s_waitcnt lgkmcnt(3)\n\t"
            "v_mfma_f32_32x32x16_bf16 %[y0], v[44:47], %[u1], %[y0]\n\t"
            "s_sleep 1\n\t"
            "v_mfma_f32_32x32x16_bf16 %[y1], v[36:39], %[u1], %[y1]\n\t"
            "ds_read2_b32 v[44:45], %[aa] offset0:88 offset1:89\n\t"
            "ds_read2_b32 v[46:47], %[aa] offset0:90 offset1:91\n\t"
            "ds_read_b128 %[u1], %[ua] offset:320\n\t"
            "s_sleep 1\n\t"
            "s_waitcnt lgkmcnt(3)\n\t"
            "v_mfma_f32_32x32x16_bf16 %[y0], v[32:35], %[u0], %[y0]\n\t"
            "s_sleep 1\n\t"
            "v_mfma_f32_32x32x16_bf16 %[y1], v[40:43], %[u0], %[y1]\n\t"
            "ds_read2_b32 v[32:33], %[aa] offset0:96 offset1:97\n\t"
            "ds_read2_b32 v[34:35], %[aa] offset0:98 offset1:99\n\t"
            "ds_read_b128 %[u0], %[ua] offset:352\n\t"
            "s_sleep 1\n\t"
            "s_waitcnt lgkmcnt(3)\n\t"
            "v_mfma_f32_32x32x16_bf16 %[y0], v[36:39], %[u1], %[y0]\n\t"
            "s_sleep 1\n\t"
            "v_mfma_f32_32x32x16_bf16 %[y1], v[44:47], %[u1], %[y1]\n\t"
            "ds_read2_b32 v[36:37], %[aa] offset0:104 offset1:105\n\t"
            "ds_read2_b32 v[38:39], %[aa] offset0:106 offset1:107\n\t"
            "ds_read_b128 %[u1], %[ua] offset:384\n\t"
            "s_sleep 1\n\t"
            "s_waitcnt lgkmcnt(3)\n\t"
            "v_mfma_f32_32x32x16_bf16 %[y0], v[40:43], %[u0], %[y0]\n\t"
            "s_sleep 1\n\t"
            "v_mfma_f32_32x32x16_bf16 %[y1], v[32:35], %[u0], %[y1]\n\t"
            "ds_read2_b32 v[40:41], %[aa] offset0:112 offset1:113\n\t"
            "ds_read2_b32 v[42:43], %[aa] offset0:114 offset1:115\n\t"
            "ds_read_b128 %[u0], %[ua] offset:144\n\t"
            "s_sleep 1\n\t"
            "s_waitcnt lgkmcnt(3)\n\t"
            "v_mfma_f32_32x32x16_bf16 %[y0], v[44:47], %[u1], %[y0]\n\t"
            "s_sleep 1\n\t"
            "v_mfma_f32_32x32x16_bf16 %[y1], v[36:39], %[u1], %[y1]\n\t"
            "ds_read2_b32 v[44:45], %[aa] offset0:120 offset1:121\n\t"
            "ds_read2_b32 v[46:47], %[aa] offset0:122 offset1:123\n\t"
            "ds_read_b128 %[u1], %[ua] offset:176\n\t"
            "s_sleep 1\n\t"
            "s_waitcnt lgkmcnt(3)\n\t"
            "v_mfma_f32_32x32x16_bf16 %[y0], v[32:35], %[u0], %[y0]\n\t"
            "s_sleep 1\n\t"
            "v_mfma_f32_32x32x16_bf16 %[y1], v[40:43], %[u0], %[y1]\n\t"
            "ds_read2_b32 v[32:33], %[aa] offset0:128 offset1:129\n\t"
            "ds_read2_b32 v[34:35], %[aa] offset0:130 offset1:131\n\t"
            "ds_read_b128 %[u0], %[ua] offset:208\n\t"
            "s_sleep 1\n\t"
            "s_waitcnt lgkmcnt(3)\n\t"
            "v_mfma_f32_32x32x16_bf16 %[y0], v[36:39], %[u1], %[y0]\n\t"
            "s_sleep 1\n\t"
            "v_mfma_f32_32x32x16_bf16 %[y1], v[44:47], %[u1], %[y1]\n\t"
            "ds_read2_b32 v[36:37], %[aa] offset0:136 offset1:137\n\t"
            "ds_read2_b32 v[38:39], %[aa] offset0:138 offset1:139\n\t"
            "ds_read_b128 %[u1], %[ua] offset:240\n\t"
            "s_sleep 1\n\t"
            "s_waitcnt lgkmcnt(3)\n\t"
            "v_mfma_f32_32x32x16_bf16 %[y0], v[40:43], %[u0], %[y0]\n\t"
            "s_sleep 1\n\t"
            "v_mfma_f32_32x32x16_bf16 %[y1], v[32:35], %[u0], %[y1]\n\t"
            "ds_read2_b32 v[40:41], %[aa] offset0:144 offset1:145\n\t"
            "ds_read2_b32 v[42:43], %[aa] offset0:146 offset1:147\n\t"
            "ds_read_b128 %[u0], %[ua] offset:0\n\t"
            "s_sleep 1\n\t"
            "s_waitcnt lgkmcnt(3)\n\t"
            "v_mfma_f32_32x32x16_bf16 %[y0], v[44:47], %[u1], %[y0]\n\t"
            "s_sleep 1\n\t"
            "v_mfma_f32_32x32x16_bf16 %[y1], v[36:39], %[u1], %[y1]\n\t"
            "ds_read2_b32 v[44:45], %[aa] offset0:152 offset1:153\n\t"
            "ds_read2_b32 v[46:47], %[aa] offset0:154 offset1:155\n\t"
            "ds_read_b128 %[u1], %[ua] offset:32\n\t"
            "s_sleep 1\n\t"
            "v_add_u32 %[aa], 512, %[aa]\n\t"
            "v_add_u32 %[ua], -576, %[ua]\n\t"
            "s_sub_u32 %[cnt], %[cnt], 1\n\t"
            "s_cmp_eq_u32 %[cnt], 5\n\t"
            "s_cbranch_scc1 5f\n\t"
            "s_cmp_eq_u32 %[cnt], 8\n\t"
            "s_cbranch_scc1 9f\n\t"
            "s_cmp_lg_u32 %[cnt], 9\n\t"
            "s_cbranch_scc1 1b\n\t"
            "7:\n\t"
            "s_mov_b32 %[t0], 0x200000\n\t"
            "2:\n\t"
            "ds_read_b32 %[tv], %[chalf]\n\t"
            "s_waitcnt lgkmcnt(0)\n\t"
            "v_readfirstlane_b32 %[t1], %[tv]\n\t"
            "s_cmp_ge_u32 %[t1], 8\n\t"
            "s_cbranch_scc1 3f\n\t"
            "s_sleep 4\n\t"
            "s_sub_u32 %[t0], %[t0], 1\n\t"
            "s_cbranch_scc0 2b\n\t"
            "3:\n\t"
            "s_mov_b32 %[t0], m0\n\t"
            "s_mov_b32 m0, %[wl]\n\t"
            "s_nop 0\n\t"
            "global_load_lds_dwordx4 %[wo0], %[wb] offset:0\n\t"
            "global_load_lds_dwordx4 %[wo1], %[wb] offset:1024\n\t"
            "global_load_lds_dwordx4 %[wo2], %[wb] offset:2048\n\t"
            "global_load_lds_dwordx4 %[wo3], %[wb] offset:3072\n\t"
            "s_add_u32 m0, m0, 4096\n\t"
            "v_add_u32 %[wo0], %[wi0], %[wo0]\n\t"
            "v_add_u32 %[wo1], %[wi0], %[wo1]\n\t"
            "v_add_u32 %[wo2], %[wi0], %[wo2]\n\t"
            "v_add_u32 %[wo3], %[wi0], %[wo3]\n\t"
            "s_nop 0\n\t"
            "global_load_lds_dwordx4 %[wo0], %[wb] offset:0\n\t"
            "global_load_lds_dwordx4 %[wo1], %[wb] offset:1024\n\t"
            "global_load_lds_dwordx4 %[wo2], %[wb] offset:2048\n\t"
            "global_load_lds_dwordx4 %[wo3], %[wb] offset:3072\n\t"
            "s_add_u32 m0, m0, 4096\n\t"
            "v_add_u32 %[wo0], %[wi1], %[wo0]\n\t"
            "v_add_u32 %[wo1], %[wi1], %[wo1]\n\t"
            "v_add_u32 %[wo2], %[wi1], %[wo2]\n\t"
            "v_add_u32 %[wo3], %[wi1], %[wo3]\n\t"
            "s_nop 0\n\t"
            "global_load_lds_dwordx4 %[wo0], %[wb] offset:0\n\t"
            "global_load_lds_dwordx4 %[wo1], %[wb] offset:1024\n\t"
            "global_load_lds_dwordx4 %[wo2], %[wb] offset:2048\n\t"
            "global_load_lds_dwordx4 %[wo3], %[wb] offset:3072\n\t"
            "s_add_u32 m0, m0, 4096\n\t"
            "v_add_u32 %[wo0], %[wi2], %[wo0]\n\t"
            "v_add_u32 %[wo1], %[wi2], %[wo1]\n\t"
            "v_add_u32 %[wo2], %[wi2], %[wo2]\n\t"
            "v_add_u32 %[wo3], %[wi2], %[wo3]\n\t"
            "s_nop 0\n\t"
            "global_load_lds_dwordx4 %[wo0], %[wb] offset:0\n\t"
            "global_load_lds_dwordx4 %[wo1], %[wb] offset:1024\n\t"
            "global_load_lds_dwordx4 %[wo2], %[wb] offset:2048\n\t"
            "global_load_lds_dwordx4 %[wo3], %[wb] offset:3072\n\t"
            "s_mov_b32 m0, %[t0]\n\t"
            "s_cmp_eq_u32 %[cnt], 9\n\t"
            "s_cbranch_scc1 1b\n\t"
            "s_branch 6f\n\t"
            "5:\n\t"
            "s_waitcnt lgkmcnt(0)\n\t"
            "ds_read_b128 %[u0], %[vz] offset:720\n\t"
            "ds_read_b128 %[u1], %[vz] offset:752\n\t"
            "ds_read_b128 %[u2], %[vb] offset:784\n\t"
            "ds_read_b128 %[u3], %[vb] offset:816\n\t"
            "s_waitcnt lgkmcnt(0)\n\t"
            "v_mfma_f32_32x32x16_bf16 %[y2], v[32:35], %[u0], %[y2]\n\t"
            "ds_read2_b32 v[32:33], %[aa] offset0:32 offset1:33\n\t"
            "ds_read2_b32 v[34:35], %[aa] offset0:34 offset1:35\n\t"
            "ds_read_b128 %[u0], %[va] offset:576\n\t"
            "s_sleep 1\n\t"
            "v_mfma_f32_32x32x16_bf16 %[y2], v[36:39], %[u1], %[y2]\n\t"
            "ds_read2_b32 v[36:37], %[aa] offset0:40 offset1:41\n\t"
            "ds_read2_b32 v[38:39], %[aa] offset0:42 offset1:43\n\t"
            "ds_read_b128 %[u1], %[va] offset:608\n\t"
            "s_sleep 1\n\t"
            "s_sub_u32 %[cnt], %[cnt], 1\n\t"
            "s_waitcnt lgkmcnt(0)\n\t"
            "6:\n\t"
            "s_waitcnt lgkmcnt(9)\n\t"
            "v_mfma_f32_32x32x16_bf16 %[y2], v[40:43], %[u2], %[y2]\n\t"
            "ds_read2_b32 v[40:41], %[aa] offset0:48 offset1:49\n\t"
            "ds_read2_b32 v[42:43], %[aa] offset0:50 offset1:51\n\t"
            "ds_read_b128 %[u2], %[vb] offset:640\n\t"
            "s_sleep 1\n\t"
            "s_waitcnt lgkmcnt(9)\n\t"
            "v_mfma_f32_32x32x16_bf16 %[y2], v[44:47], %[u3], %[y2]\n\t"
            "ds_read2_b32 v[44:45], %[aa] offset0:56 offset1:57\n\t"
            "ds_read2_b32 v[46:47], %[aa] offset0:58 offset1:59\n\t"
            "ds_read_b128 %[u3], %[vb] offset:672\n\t"
            "s_sleep 1\n\t"
            "s_waitcnt lgkmcnt(9)\n\t"
            "v_mfma_f32_32x32x16_bf16 %[y2], v[32:35], %[u0], %[y2]\n\t"
            "ds_read2_b32 v[32:33], %[aa] offset0:64 offset1:65\n\t"
            "ds_read2_b32 v[34:35], %[aa] offset0:66 offset1:67\n\t"
            "ds_read_b128 %[u0], %[va] offset:432\n\t"
            "s_sleep 1\n\t"
            "s_waitcnt lgkmcnt(9)\n\t"
            "v_mfma_f32_32x32x16_bf16 %[y2], v[36:39], %[u1], %[y2]\n\t"
            "ds_read2_b32 v[36:37], %[aa] offset0:72 offset1:73\n\t"
            "ds_read2_b32 v[38:39], %[aa] offset0:74 offset1:75\n\t"
            "ds_read_b128 %[u1], %[va] offset:464\n\t"
            "s_sleep 1\n\t"
            "s_waitcnt lgkmcnt(9)\n\t"
            "v_mfma_f32_32x32x16_bf16 %[y2], v[40:43], %[u2], %[y2]\n\t"
            "ds_read2_b32 v[40:41], %[aa] offset0:80 offset1:81\n\t"
            "ds_read2_b32 v[42:43], %[aa] offset0:82 offset1:83\n\t"
            "ds_read_b128 %[u2], %[vb] offset:496\n\t"
            "s_sleep 1\n\t"
            "s_waitcnt lgkmcnt(9)\n\t"
            "v_mfma_f32_32x32x16_bf16 %[y2], v[44:47], %[u3], %[y2]\n\t"
            "ds_read2_b32 v[44:45], %[aa] offset0:88 offset1:89\n\t"
            "ds_read2_b32 v[46:47], %[aa] offset0:90 offset1:91\n\t"
            "ds_read_b128 %[u3], %[vb] offset:528\n\t"
            "s_sleep 1\n\t"
            "s_waitcnt lgkmcnt(9)\n\t"
            "v_mfma_f32_32x32x16_bf16 %[y2], v[32:35], %[u0], %[y2]\n\t"
            "ds_read2_b32 v[32:33], %[aa] offset0:96 offset1:97\n\t"
            "ds_read2_b32 v[34:35], %[aa] offset0:98 offset1:99\n\t"
            "ds_read_b128 %[u0], %[va] offset:288\n\t"
            "s_sleep 1\n\t"
            "s_waitcnt lgkmcnt(9)\n\t"
            "v_mfma_f32_32x32x16_bf16 %[y2], v[36:39], %[u1], %[y2]\n\t"
            "ds_read2_b32 v[36:37], %[aa] offset0:104 offset1:105\n\t"
            "ds_read2_b32 v[38:39], %[aa] offset0:106 offset1:107\n\t"
            "ds_read_b128 %[u1], %[va] offset:320\n\t"
            "s_sleep 1\n\t"
            "s_waitcnt lgkmcnt(9)\n\t"
            "v_mfma_f32_32x32x16_bf16 %[y2], v[40:43], %[u2], %[y2]\n\t"
            "ds_read2_b32 v[40:41], %[aa] offset0:112 offset1:113\n\t"
            "ds_read2_b32 v[42:43], %[aa] offset0:114 offset1:115\n\t"
            "ds_read_b128 %[u2], %[vb] offset:352\n\t"
            "s_sleep 1\n\t"
            "s_waitcnt lgkmcnt(9)\n\t"
            "v_mfma_f32_32x32x16_bf16 %[y2], v[44:47], %[u3], %[y2]\n\t"
            "ds_read2_b32 v[44:45], %[aa] offset0:120 offset1:121\n\t"
            "ds_read2_b32 v[46:47], %[aa] offset0:122 offset1:123\n\t"
            "ds_read_b128 %[u3], %[vb] offset:384\n\t"
            "s_sleep 1\n\t"
            "s_waitcnt lgkmcnt(9)\n\t"
            "v_mfma_f32_32x32x16_bf16 %[y2], v[32:35], %[u0], %[y2]\n\t"
            "ds_read2_b32 v[32:33], %[aa] offset0:128 offset1:129\n\t"
            "ds_read2_b32 v[34:35], %[aa] offset0:130 offset1:131\n\t"
            "ds_read_b128 %[u0], %[va] offset:144\n\t"
            "s_sleep 1\n\t"
            "s_waitcnt lgkmcnt(9)\n\t"
            "v_mfma_f32_32x32x16_bf16 %[y2], v[36:39], %[u1], %[y2]\n\t"
            "ds_read2_b32 v[36:37], %[aa] offset0:136 offset1:137\n\t"
            "ds_read2_b32 v[38:39], %[aa] offset0:138 offset1:139\n\t"
            "ds_read_b128 %[u1], %[va] offset:176\n\t"
            "s_sleep 1\n\t"
            "s_waitcnt lgkmcnt(9)\n\t"
            "v_mfma_f32_32x32x16_bf16 %[y2], v[40:43], %[u2], %[y2]\n\t"
            "ds_read2_b32 v[40:41], %[aa] offset0:144 offset1:145\n\t"
            "ds_read2_b32 v[42:43], %[aa] offset0:146 offset1:147\n\t"
            "ds_read_b128 %[u2], %[vb] offset:208\n\t"
            "s_sleep 1\n\t"
            "s_waitcnt lgkmcnt(9)\n\t"
            "v_mfma_f32_32x32x16_bf16 %[y2], v[44:47], %[u3], %[y2]\n\t"
            "ds_read2_b32 v[44:45], %[aa] offset0:152 offset1:153\n\t"
            "ds_read2_b32 v[46:47], %[aa] offset0:154 offset1:155\n\t"
            "ds_read_b128 %[u3], %[vb] offset:240\n\t"
            "s_sleep 1\n\t"
            "s_waitcnt lgkmcnt(9)\n\t"
            "v_mfma_f32_32x32x16_bf16 %[y2], v[32:35], %[u0], %[y2]\n\t"
            "ds_read2_b32 v[32:33], %[aa] offset0:160 offset1:161\n\t"
            "ds_read2_b32 v[34:35], %[aa] offset0:162 offset1:163\n\t"
            "ds_read_b128 %[u0], %[va] offset:0\n\t"
            "s_sleep 1\n\t"
            "s_waitcnt lgkmcnt(9)\n\t"
            "v_mfma_f32_32x32x16_bf16 %[y2], v[36:39], %[u1], %[y2]\n\t"
            "ds_read2_b32 v[36:37], %[aa] offset0:168 offset1:169\n\t"
            "ds_read2_b32 v[38:39], %[aa] offset0:170 offset1:171\n\t"
            "ds_read_b128 %[u1], %[va] offset:32\n\t"
            "s_sleep 1\n\t"
            "v_add_u32 %[aa], 512, %[aa]\n\t"
            "v_add_u32 %[va], -576, %[va]\n\t"
            "v_add_u32 %[vb], -576, %[vb]\n\t"
            "s_sub_u32 %[cnt], %[cnt], 1\n\t"
            "s_cmp_eq_u32 %[cnt], 2\n\t"
            "s_cbranch_scc1 7b\n\t"
            "s_cmp_lg_u32 %[cnt], 0\n\t"
            "s_cbranch_scc1 6b\n\t"
            "9:\n\t"
            "s_waitcnt lgkmcnt(0)\n\t"
            "s_nop 11\n\t"
            : [y0] "+v"(y0), [y1] "+v"(y1), [y2] "+v"(y2), [u0] "=&v"(u0), [u1] "=&v"(u1), [u2] "=&v"(u2), [u3] "=&v"(u3),
              [aa] "+v"(aa), [ua] "+v"(ua), [va] "+v"(va), [vb] "+v"(vb), [cnt] "+s"(cnt), [t0] "=&s"(t0), [t1] "=&s"(t1),
              [tv] "=&v"(tv), [wo0] "+v"(wp0), [wo1] "+v"(wp1), [wo2] "+v"(wp2), [wo3] "+v"(wp3)
            : [vz] "v"(vz), [chalf] "v"(C_HALF), [wb] "s"(wob), [wl] "s"(wol), [wi0] "s"(wi0), [wi1] "s"(wi1), [wi2] "s"(wi2)
            : "memory", "scc", "v32", "v33", "v34", "v35", "v36", "v37", "v38", "v39", "v40", "v41", "v42", "v43", "v44", "v45", "v46", "v47");
      }
    }
    if (kh) {
#pragma unroll
      for (int i = 0; i < 16; ++i) {
        auto t = __builtin_amdgcn_permlane16_swap(__float_as_uint(y2[i]), 0u, false, false);
        y0[i] += __uint_as_float(t[0]);
        y1[i] += __uint_as_float(t[1]);
      }
    }
    STAMP(8);
    lds_signal(C_CP + cl * 4, lane);
    const rsrc_t qq = mk_rsrc(p.bq, 2048 * 1024);
    qv[0] = buf_ld128(qq, qlo);
    qv[1] = buf_ld128(qq, qlo, 32768);
    lds_wait<0>(C_CP + cl * 4, 2);
    auto put = [&](const f32x16& v) {
#pragma unroll
      for (int k = 0; k < 4; ++k) lds_wr128(exw + k * 1024, __builtin_bit_cast(u32x4, f32x4{v[4 * k], v[4 * k + 1], v[4 * k + 2], v[4 * k + 3]}));
    };
    auto fin = [&](const f32x16& y) {
      u32x2 z[4];
#pragma unroll
      for (int k = 0; k < 4; ++k) {
        const f32x4 o = __builtin_bit_cast(f32x4, lds_rd128(exr + k * 1024));
        const u32x2 u = lds_rd64(urd + 48 - 16 * k);
        z[k].x = pack2(y[4 * k] + o[0] + bf_hi(u.y) * dd, y[4 * k + 1] + o[1] + bf_lo(u.y) * dd);
        z[k].y = pack2(y[4 * k + 2] + o[2] + bf_hi(u.x) * dd, y[4 * k + 3] + o[3] + bf_lo(u.x) * dd);
      }
      wait_lgkm0();
#pragma unroll
      for (int k = 0; k < 4; ++k) *(LAS u32x2*)(uintptr_t)(uwr + 16 * k) = z[k];
    };
    if (kh) put(y0); else put(y1);
    lds_signal(C_CP + cl * 4, lane);
    lds_wait<0>(C_CP + cl * 4, 4);
    if (kh) fin(y1); else fin(y0);
    lds_signal(C_CP + cl * 4, lane);
    lds_wait<0>(C_CP + cl * 4, 6);
    const u32x4 za = lds_rd128(zr0), zb = lds_rd128(zr1);
    lds_signal(C_CD, lane);
    buf_st128_dev(ws, zso, 65536, za);
    buf_st128_dev(ws, zso, 0, zb);
    publish_join(ws, C_YJ, 4, WS_FY + (dg * 32 + j) * 128, epoch << 16, lane);
  }
  {
#pragma unroll
    for (int i = 0; i < 16; ++i) wf[i] = lds_tr2(wa + i * 1024, 512);
#pragma unroll
    for (int i = 0; i < 16; ++i) pin(wf[i]);
#pragma unroll
    for (int i = 0; i < 16; ++i) dma16(wob + (256 + 16 * i) * 1024, wo0, wol + i * 1024);
  }
  STAMP(9);
  const rsrc_t outr = mk_rsrc(p.out, 2048 * 2048);
  {
    if (w == 0) {
      wait_flags<1, 8>(ws, WS_FY + (dg * 32 + l31) * 128, 0, epoch);
      lds_signal(C_FY, lane);
    } else {
      lds_wait<4>(C_FY, 1);
    }
    STAMP(10);
    const u32x4 z0 = buf_ld128_dev(ws, zlo), z1 = buf_ld128_dev(ws, zlo + 1024);
    lds_wr128(ztw, z0);
    lds_wr128(ztw + 1024, z1);
    wait_lgkm0();
#pragma unroll
    for (int r = 0; r < 2; ++r) {
      const u32x2 a = lds_tr(ztr + 64 * r), bb = lds_tr(ztr + 64 * r + 512);
      const f32x2 p0 = f32x2{bf_lo(qv[r].x), bf_hi(qv[r].x)} * f32x2{bf_lo(a.x), bf_hi(a.x)};
      const f32x2 p1 = f32x2{bf_lo(qv[r].y), bf_hi(qv[r].y)} * f32x2{bf_lo(a.y), bf_hi(a.y)};
      const f32x2 p2 = f32x2{bf_lo(qv[r].z), bf_hi(qv[r].z)} * f32x2{bf_lo(bb.x), bf_hi(bb.x)};
      const f32x2 p3 = f32x2{bf_lo(qv[r].w), bf_hi(qv[r].w)} * f32x2{bf_lo(bb.y), bf_hi(bb.y)};
      buf_st128(outr, gso, 65536 * r, u32x4{pack2(p0.x, p0.y), pack2(p1.x, p1.y), pack2(p2.x, p2.y), pack2(p3.x, p3.y)});
    }
  }
  STAMP(11);
  {
    unsigned oa = L_O + l16;
    pin(oa);
    lds_wait<4>(L_MISC + 116, 8);
    wait_flags<1, 1>(ws, WS_FO + (qb * 8 + c8) * 128, 0, epoch);
    const char* ob = (const char*)p.ws + WS_O + (2 * qb + (hg & 1)) * 32768 + w * 4096;
    dma16x4_dev(ob, l16, L_O + w * 4096);
    dma16x4_dev(ob + 16384, l16, L_O + 16384 + w * 4096);
    f32x16 acc = {};
    u32x4 of[16];
    asm volatile("s_waitcnt vmcnt(4)\n\ts_barrier" ::: "memory");
#pragma unroll
    for (int i = 0; i < 16; ++i) of[i] = lds_rd128(oa + i * 1024);
    fence_sched();
#pragma unroll
    for (int i = 0; i < 16; ++i) {
      acc = __builtin_amdgcn_mfma_f32_32x32x16_bf16(as_bf8(wf[i]), as_bf8(of[i]), acc, 0, 0, 0);
      pin(acc);
      wf[i] = lds_tr2(wa + i * 1024, 512);
      fence_sched();
    }
    asm volatile("s_waitcnt vmcnt(0)\n\ts_barrier" ::: "memory");
#pragma unroll
    for (int i = 0; i < 16; ++i) of[i] = lds_rd128(oa + 16384 + i * 1024);
    fence_sched();
#pragma unroll
    for (int i = 0; i < 16; ++i) acc = __builtin_amdgcn_mfma_f32_32x32x16_bf16(as_bf8(wf[i]), as_bf8(of[i]), acc, 0, 0, 0);
#pragma unroll
    for (int k = 0; k < 4; k += 2) {
      auto sx = __builtin_amdgcn_permlane32_swap(pack2(acc[4 * k], acc[4 * k + 1]), pack2(acc[4 * k + 4], acc[4 * k + 5]), false, false);
      auto sy = __builtin_amdgcn_permlane32_swap(pack2(acc[4 * k + 2], acc[4 * k + 3]), pack2(acc[4 * k + 6], acc[4 * k + 7]), false, false);
      buf_st128(outr, aso + 16 * k, 0, u32x4{sx[0], sy[0], sx[1], sy[1]});
    }
  }
  STAMP(15);
  STAMP_DUMP();
}

extern "C" void kernel_launch(void* const* d_in, const int* in_sizes, int n_in, void* d_out, int out_size, void* d_ws,
                              size_t ws_size, hipStream_t stream) {
  Params p;
  p.x = d_in[0]; p.wq = d_in[1]; p.wk = d_in[2]; p.wv = d_in[3]; p.wo = d_in[4];
  p.bq = d_in[5]; p.bk = d_in[6]; p.bv = d_in[7]; p.shk = d_in[8]; p.ssk = d_in[9]; p.ssd = d_in[10];
  p.out = d_out; p.ws = d_ws;
  fused_kernel<<<256, 768, 0, stream>>>(p);
}
```
